```python
import torch

def _softmax(x, axis=-1):
    return torch.softmax(x, dim=axis)
def _gelu(x):
    return torch.nn.functional.gelu(x, approximate="tanh")
def _silu(x):
    return torch.nn.functional.silu(x)
def _sigmoid(x):
    return torch.sigmoid(x)
def _layernorm(x, axis=-1, eps=1e-5):
    m = x.mean(dim=axis, keepdim=True)
    v = x.var(dim=axis, keepdim=True, unbiased=False)
    return (x - m) / torch.sqrt(v + eps)
def _scatter_add(tgt, idx, src, axis=0):
    return tgt.clone().index_add_(axis, idx, src)
def _segment_sum(src, seg, n):
    out = torch.zeros((n,) + src.shape[1:], dtype=src.dtype)
    return out.index_add_(0, seg, src)
def _pad(x, pw):
    flat = tuple(v for pair in reversed(pw) for v in pair)
    return torch.nn.functional.pad(x, flat)

kernel_name = 'standalone__mpnn_edge_mlp__graph'

def setup_inputs():

    _w = (lambda a: (lambda t: t.to(torch.bfloat16) if t.is_floating_point() else t)(torch.as_tensor(a)))
    N, deg, D, De = 50000, 16, 64, max(16, 1)
    E = N * deg
    return {
        "x": _w((lambda *s: torch.randn(*s, dtype=torch.bfloat16) * 0.05)(N, D)),
        "e_attr": _w((lambda *s: torch.randn(*s, dtype=torch.bfloat16) * 0.4)(E, De)),
        "src": _w((lambda lo, hi, *s: torch.randint(lo, hi, s))(0, N, E).to(torch.int64)),
        "dst": _w((lambda lo, hi, *s: torch.randint(lo, hi, s))(0, N, E).to(torch.int64)),
        "w_msg": _w((lambda *s: torch.randn(*s, dtype=torch.bfloat16) * 0.4)(2 * D + De, D)),
        "w_upd": _w((lambda *s: torch.randn(*s, dtype=torch.bfloat16) * 0.05)(2 * D, D)),
    }

def reference(x, e_attr, src, dst, w_msg, w_upd):
    N, D = x.shape
    m_in = torch.concatenate([x[src], x[dst], e_attr], axis=-1)
    msg = _silu(m_in @ w_msg)
    agg = _segment_sum(msg, dst, N)
    return _silu(torch.concatenate([x, agg], axis=-1) @ w_upd)
```

```cpp
#include <hip/hip_runtime.h>
#include <hip/hip_bf16.h>
#include <stdint.h>

static __device__ __forceinline__ float bf2f(uint16_t v) { return __uint_as_float(((uint32_t)v) << 16); }
static __device__ __forceinline__ uint16_t f2bf(float f) { uint32_t u = __float_as_uint(f); u += 0x7FFF + ((u >> 16) & 1); return (uint16_t)(u >> 16); }

__global__ void k_zero(float* agg, int n) { int i = blockIdx.x * blockDim.x + threadIdx.x; if (i < n) agg[i] = 0.f; }

__global__ __launch_bounds__(256) void k_edge(const uint16_t* x, const uint16_t* ea, const int* src, const int* dst, const uint16_t* wm, float* agg, int E) {
  __shared__ float w[144 * 64];
  for (int i = threadIdx.x; i < 144 * 64; i += blockDim.x) w[i] = bf2f(wm[i]);
  __syncthreads();
  int lane = threadIdx.x & 63, wv = threadIdx.x >> 6;
  for (int e = blockIdx.x * 4 + wv; e < E; e += gridDim.x * 4) {
    int s = src[e], d = dst[e];
    float acc = 0.f;
    _Pragma("unroll 4") for (int k = 0; k < 64; ++k) acc += bf2f(x[(size_t)s * 64 + k]) * w[k * 64 + lane];
    _Pragma("unroll 4") for (int k = 0; k < 64; ++k) acc += bf2f(x[(size_t)d * 64 + k]) * w[(64 + k) * 64 + lane];
    _Pragma("unroll 4") for (int k = 0; k < 16; ++k) acc += bf2f(ea[(size_t)e * 16 + k]) * w[(128 + k) * 64 + lane];
    float p = bf2f(f2bf(acc));
    float m = p / (1.f + __expf(-p));
    atomicAdd(&agg[(size_t)d * 64 + lane], bf2f(f2bf(m)));
  }
}
__global__ __launch_bounds__(256) void k_upd(const uint16_t* x, const float* agg, const uint16_t* wu, uint16_t* out, int N) {
  __shared__ float w[128 * 64];
  for (int i = threadIdx.x; i < 128 * 64; i += blockDim.x) w[i] = bf2f(wu[i]);
  __syncthreads();
  int lane = threadIdx.x & 63, wv = threadIdx.x >> 6;
  for (int n = blockIdx.x * 4 + wv; n < N; n += gridDim.x * 4) {
    float acc = 0.f;
    _Pragma("unroll 4") for (int k = 0; k < 64; ++k) acc += bf2f(x[(size_t)n * 64 + k]) * w[k * 64 + lane];
    _Pragma("unroll 4") for (int k = 0; k < 64; ++k) acc += bf2f(f2bf(agg[(size_t)n * 64 + k])) * w[(64 + k) * 64 + lane];
    float p = bf2f(f2bf(acc));
    out[(size_t)n * 64 + lane] = f2bf(p / (1.f + __expf(-p)));
  }
}

extern "C" void kernel_launch(void* const* d_in, const int* in_sizes, int n_in, void* d_out, int out_size, void* d_ws, size_t ws_size, hipStream_t stream) {
  const uint16_t* x = (const uint16_t*)d_in[0];
  const uint16_t* ea = (const uint16_t*)d_in[1];
  const int* src = (const int*)d_in[2];
  const int* dst = (const int*)d_in[3];
  const uint16_t* wm = (const uint16_t*)d_in[4];
  const uint16_t* wu = (const uint16_t*)d_in[5];
  int N = in_sizes[0] / 64, E = in_sizes[2];
  float* agg = (float*)d_ws;
  k_zero<<<(N * 64 + 255) / 256, 256, 0, stream>>>(agg, N * 64);
  k_edge<<<2048, 256, 0, stream>>>(x, ea, src, dst, wm, agg, E);
  k_upd<<<1024, 256, 0, stream>>>(x, agg, wu, (uint16_t*)d_out, N);
}
```

```cpp
#include <hip/hip_runtime.h>
#include <stdint.h>

#define LDS __attribute__((address_space(3)))
#define DI __device__ __forceinline__
typedef float f32x16 __attribute__((ext_vector_type(16)));
typedef float f32x2 __attribute__((ext_vector_type(2)));
typedef unsigned u32x4 __attribute__((ext_vector_type(4)));
typedef unsigned u32x2 __attribute__((ext_vector_type(2)));
typedef int i32x4 __attribute__((ext_vector_type(4)));
typedef __bf16 bf16x8 __attribute__((ext_vector_type(8)));
typedef __bf16 bf16x2 __attribute__((ext_vector_type(2)));
typedef short s16x4 __attribute__((ext_vector_type(4)));

constexpr int NN = 50000;
constexpr int NE = 800000;
constexpr int GP = 25;
constexpr int CE = 3200;
constexpr int NC = 250;
constexpr int NB = 250;
constexpr int CAP = 512;
constexpr unsigned WS_EP = 0, WS_META = 25600000u, WS_OFF = 28800000u, WS_WF = 29824000u;

#define MFMA(a, b, c) __builtin_amdgcn_mfma_f32_32x32x16_bf16((a), (b), (c), 0, 0, 0)
#define BAR() asm volatile("s_waitcnt lgkmcnt(0)\n\ts_barrier" ::: "memory")
#define SB() __builtin_amdgcn_sched_barrier(0)

DI unsigned pk(float a, float b) { f32x2 v = {a, b}; bf16x2 r = __builtin_convertvector(v, bf16x2); return __builtin_bit_cast(unsigned, r); }
DI unsigned wave_incl_scan(unsigned x) {
  x += __builtin_amdgcn_update_dpp(0u, x, 0x111, 0xf, 0xf, false);
  x += __builtin_amdgcn_update_dpp(0u, x, 0x112, 0xf, 0xf, false);
  x += __builtin_amdgcn_update_dpp(0u, x, 0x114, 0xf, 0xf, false);
  x += __builtin_amdgcn_update_dpp(0u, x, 0x118, 0xf, 0xf, false);
  x += __builtin_amdgcn_update_dpp(0u, x, 0x142, 0xa, 0xf, false);
  x += __builtin_amdgcn_update_dpp(0u, x, 0x143, 0xc, 0xf, false);
  return x;
}

__global__ __launch_bounds__(1024) void k_part(const i32x4* __restrict__ dst4, const i32x4* __restrict__ src4, const u32x4* __restrict__ e16,
                                               const uint16_t* __restrict__ wm, const uint16_t* __restrict__ wu, char* __restrict__ ws) {
  __shared__ unsigned cnt[2048];
  __shared__ __attribute__((aligned(16))) unsigned metaL[CE];
  __shared__ __attribute__((aligned(16))) unsigned short posL[CE];
  __shared__ unsigned wsum[16];
  const int tid = threadIdx.x, c = blockIdx.x;
  if (c >= NC) {
    const int it = (c - NC) * 1024 + tid;
    if (it < 34 * 64) {
      const int f = it >> 6, l = it & 63, col = l & 31, h = l >> 5;
      unsigned short v[8];
#pragma unroll
      for (int j = 0; j < 8; ++j) {
        if (f < 18) {
          const int t = f & 1, s = (f >> 1) & 3;
          const int row = (f < 8 ? 0 : (f < 16 ? 64 : 128)) + (f < 16 ? 16 * s : 0) + 8 * h + j;
          v[j] = wm[row * 64 + 32 * t + col];
        } else {
          const int ks = (f - 18) >> 1, u = f & 1;
          const int row = ks < 4 ? 16 * ks + 8 * h + j : 64 + 32 * ((ks - 4) >> 1) + 16 * (ks & 1) + 8 * (j >> 2) + 4 * h + (j & 3);
          v[j] = wu[row * 64 + 32 * u + col];
        }
      }
      u32x4 o = {v[0] | ((unsigned)v[1] << 16), v[2] | ((unsigned)v[3] << 16), v[4] | ((unsigned)v[5] << 16), v[6] | ((unsigned)v[7] << 16)};
      *(u32x4*)(ws + WS_WF + it * 16) = o;
    }
    return;
  }
  i32x4 d = {0, 0, 0, 0}, s = {0, 0, 0, 0};
  if (tid < CE / 4) { d = dst4[c * (CE / 4) + tid]; s = src4[c * (CE / 4) + tid]; }
  u32x4 ev[7];
#pragma unroll
  for (int i = 0; i < 6; ++i) ev[i] = __builtin_nontemporal_load(e16 + c * (CE * 2) + tid + 1024 * i);
  ev[6] = u32x4{0, 0, 0, 0};
  if (tid < CE * 2 - 6144) ev[6] = __builtin_nontemporal_load(e16 + c * (CE * 2) + tid + 6144);
  cnt[tid] = 0; cnt[tid + 1024] = 0;
  BAR();
  unsigned g[4], rk[4];
  if (tid < CE / 4) {
#pragma unroll
    for (int j = 0; j < 4; ++j) { g[j] = (unsigned)d[j] / (unsigned)GP; rk[j] = __hip_atomic_fetch_add(&cnt[g[j]], 1u, __ATOMIC_RELAXED, __HIP_MEMORY_SCOPE_WORKGROUP); }
  }
  BAR();
  const u32x2 cc = *(const u32x2*)&cnt[2 * tid];
  const unsigned incl = wave_incl_scan(cc[0] + cc[1]);
  if ((tid & 63) == 63) wsum[tid >> 6] = incl;
  BAR();
  unsigned ws16 = wsum[tid & 15];
  ws16 += __builtin_amdgcn_update_dpp(0u, ws16, 0x111, 0xf, 0xf, false);
  ws16 += __builtin_amdgcn_update_dpp(0u, ws16, 0x112, 0xf, 0xf, false);
  ws16 += __builtin_amdgcn_update_dpp(0u, ws16, 0x114, 0xf, 0xf, false);
  ws16 += __builtin_amdgcn_update_dpp(0u, ws16, 0x118, 0xf, 0xf, false);
  const int w = __builtin_amdgcn_readfirstlane(tid >> 6);
  const unsigned wb = w ? __builtin_amdgcn_readlane(ws16, w - 1) : 0u;
  const unsigned o0 = wb + incl - cc[0] - cc[1], o1 = o0 + cc[0];
  *(u32x2*)&cnt[2 * tid] = u32x2{o0, o1};
  *(unsigned*)(ws + WS_OFF + c * 4096 + tid * 4) = o0 | (o1 << 16);
  BAR();
  if (tid < CE / 4) {
    unsigned p[4];
#pragma unroll
    for (int j = 0; j < 4; ++j) {
      p[j] = cnt[g[j]] + rk[j];
      metaL[p[j]] = ((unsigned)s[j] << 7) | (((unsigned)d[j] - g[j] * GP) << 27);
    }
    *(u32x2*)&posL[4 * tid] = u32x2{p[0] | (p[1] << 16), p[2] | (p[3] << 16)};
  }
  BAR();
  if (tid < CE / 4) *(u32x4*)(ws + WS_META + c * (CE * 4) + tid * 16) = *(const u32x4*)&metaL[4 * tid];
  char* ep = ws + WS_EP + c * (CE * 32);
#pragma unroll
  for (int i = 0; i < 7; ++i) {
    const int p = tid + 1024 * i;
    if (i < 6 || tid < CE * 2 - 6144) *(u32x4*)(ep + posL[p >> 1] * 32 + (p & 1) * 16) = ev[i];
  }
}

constexpr unsigned L_STG = 0;
constexpr unsigned L_S = 81920;
constexpr unsigned L_LIST = 98304;
constexpr unsigned L_WU = 131072;
constexpr unsigned L_T1 = 147456;
constexpr unsigned L_T2 = 151552;
constexpr unsigned L_END = 152064;

DI void dma16(unsigned m0, unsigned voff, __amdgpu_buffer_rsrc_t srd, unsigned soff) {
  asm volatile("s_mov_b32 m0, %0\n\ts_nop 0\n\tbuffer_load_dwordx4 %1, %2, %3 offen lds" ::"s"(m0), "v"(voff), "s"(srd), "s"(soff) : "m0", "memory");
}
DI float silu1(float v) { return v * __builtin_amdgcn_rcpf(1.0f + __builtin_amdgcn_exp2f(v * -1.4426950408889634f)); }

__global__ __launch_bounds__(512, 2) void k_main(const char* __restrict__ x, const char* __restrict__ ws, char* __restrict__ out) {
  __shared__ __attribute__((aligned(1024))) char lds_[L_END];
  LDS char* lds = (LDS char*)lds_;
  const unsigned ldsb = (unsigned)(size_t)lds;
  const int tid = threadIdx.x, l = tid & 63, w = __builtin_amdgcn_readfirstlane(tid >> 6);
  const int xcd = blockIdx.x & 7, b = xcd * 31 + (xcd < 2 ? xcd : 2) + (blockIdx.x >> 3);
  const int g = 8 * b + w;
  const int r = l & 31, h = l >> 5;
  const auto sx = __builtin_amdgcn_make_buffer_rsrc((void*)x, 0, NN * 128, 0x00020000);
  const auto se = __builtin_amdgcn_make_buffer_rsrc((void*)(ws + WS_EP), 0, NE * 32, 0x00020000);
  const auto sm = __builtin_amdgcn_make_buffer_rsrc((void*)(ws + WS_META), 0, NE * 4, 0x00020000);
  const auto sw = __builtin_amdgcn_make_buffer_rsrc((void*)(ws + WS_WF), 0, 34 * 1024, 0x00020000);

  u32x4 t1 = {0, 0, 0, 0}; unsigned short t2 = 0;
  if (tid < NC) { const char* row = ws + WS_OFF + tid * 4096 + b * 16; t1 = *(const u32x4*)row; t2 = *(const unsigned short*)(row + 16); }
  const unsigned stg = L_STG + w * 10240;
  asm volatile("s_nop 4" ::: "memory");
  for (int f = w; f < 34; f += 8) {
    const unsigned dstl = f < 18 ? L_STG + (f / 5) * 10240 + 5120 + (f % 5) * 1024 : L_WU + (f - 18) * 1024;
    dma16(ldsb + dstl, l * 16, sw, f * 1024);
  }
#pragma unroll
  for (int s = 0; s < 4; ++s) {
    const int R = 8 * s + (l >> 3);
    int node = GP * g + R; node = node < NN ? node : NN - 1;
    dma16(ldsb + stg + s * 1024, node * 128 + (((l & 7) ^ ((R >> 1) & 7)) << 4), sx, 0);
  }
  const unsigned sreg = L_S + w * 2048;
  *(LDS u32x4*)(lds + sreg + l * 16) = u32x4{0, 0, 0, 0};
  *(LDS u32x4*)(lds + sreg + 1024 + l * 16) = u32x4{0, 0, 0, 0};
  if (tid < NC) { *(LDS u32x4*)(lds + L_T1 + tid * 16) = t1; *(LDS unsigned short*)(lds + L_T2 + tid * 2) = t2; }
  asm volatile("s_waitcnt vmcnt(0)" ::: "memory");
  BAR();

  bf16x8 W1[4][2], W3[2], P2[2][2], XO[4];
  {
    auto wfrag = [&](int f) { return *(LDS bf16x8*)(lds + L_STG + (f / 5) * 10240 + 5120 + (f % 5) * 1024 + l * 16); };
#pragma unroll
    for (int s = 0; s < 4; ++s) { W1[s][0] = wfrag(2 * s); W1[s][1] = wfrag(2 * s + 1); }
    W3[0] = wfrag(16); W3[1] = wfrag(17);
    const int rs = (r & 19) | ((r & 4) << 1) | ((r & 8) >> 1);
    f32x16 p0, p1;
#pragma unroll
    for (int i = 0; i < 16; ++i) { p0[i] = 0.f; p1[i] = 0.f; }
#pragma unroll
    for (int s = 0; s < 4; ++s) {
      const bf16x8 a = *(LDS bf16x8*)(lds + stg + rs * 128 + (((2 * s + h) ^ ((rs >> 1) & 7)) << 4));
      p0 = MFMA(a, wfrag(8 + 2 * s), p0);
      p1 = MFMA(a, wfrag(9 + 2 * s), p1);
      XO[s] = *(LDS bf16x8*)(lds + stg + r * 128 + (((2 * s + h) ^ ((r >> 1) & 7)) << 4));
    }
#pragma unroll
    for (int s = 0; s < 2; ++s) {
      P2[s][0] = __builtin_bit_cast(bf16x8, u32x4{pk(p0[8 * s], p0[8 * s + 1]), pk(p0[8 * s + 2], p0[8 * s + 3]), pk(p0[8 * s + 4], p0[8 * s + 5]), pk(p0[8 * s + 6], p0[8 * s + 7])});
      P2[s][1] = __builtin_bit_cast(bf16x8, u32x4{pk(p1[8 * s], p1[8 * s + 1]), pk(p1[8 * s + 2], p1[8 * s + 3]), pk(p1[8 * s + 4], p1[8 * s + 5]), pk(p1[8 * s + 6], p1[8 * s + 7])});
    }
  }
  f32x16 Y0, Y1;
#pragma unroll
  for (int i = 0; i < 16; ++i) { Y0[i] = 0.f; Y1[i] = 0.f; }

  const unsigned la = L_LIST + w * 4096, lb = la + 2048;
  unsigned xch[4];
#pragma unroll
  for (int s = 0; s < 4; ++s) xch[s] = ((l & 7) ^ (((8 * s + (l >> 3)) >> 1) & 7)) << 4;
  const unsigned ech = ((l & 1) ^ ((l >> 4) & 1)) << 4;
  const unsigned skey = sreg + (l >> 1) * 64 + (((l >> 3) & 3) << 4);
  const unsigned aoff = la + (l >> 3) * 16;
  const unsigned boff = lb + (4 * ((l >> 1) & 7) + (l >> 4)) * 4;
  unsigned xrd[4];
#pragma unroll
  for (int s = 0; s < 4; ++s) xrd[s] = stg + r * 128 + (((2 * s + h) ^ ((r >> 1) & 7)) << 4);
  const unsigned erd = stg + 4096 + r * 32 + ((h ^ ((r >> 3) & 1)) << 4);
  const unsigned srow0 = sreg + r * 64 + ((h ^ ((r >> 2) & 3)) << 4), srow1 = srow0 ^ 32;
  const unsigned str0 = sreg + (4 * h + ((l & 15) >> 2)) * 64 + (((2 * ((l >> 4) & 1) + ((l & 3) >> 1)) ^ h) << 4) + (l & 1) * 8;
  const unsigned str1 = (str0 ^ 32) + 512;

  int wb = 0, n;
  bool first = true;
  do {
    unsigned cn[4], rb[4], ix[4];
    unsigned tot = 0;
#pragma unroll
    for (int k = 0; k < 4; ++k) {
      const int c = l + 64 * k;
      const int cc = c < NC ? c : 0;
      const unsigned o0 = *(LDS unsigned short*)(lds + L_T1 + cc * 16 + w * 2);
      const unsigned o1 = *(LDS unsigned short*)(lds + (w < 7 ? L_T1 + cc * 16 + w * 2 + 2 : L_T2 + cc * 2));
      cn[k] = c < NC ? o1 - o0 : 0u;
      rb[k] = cc * CE + o0;
      ix[k] = tot;
      tot += cn[k];
    }
    const unsigned incl = wave_incl_scan(tot);
    n = __builtin_amdgcn_readlane(incl, 63);
#pragma unroll
    for (int k = 0; k < 4; ++k) ix[k] += incl - tot;
    const int nw = n - wb < CAP ? n - wb : CAP;
    const int nt = (nw + 31) >> 5;
    {
      int j0[4], j1[4], m = 0;
#pragma unroll
      for (int k = 0; k < 4; ++k) {
        const int lo = (int)ix[k] - wb;
        j0[k] = lo < 0 ? -lo : 0;
        j1[k] = (int)cn[k] < CAP - lo ? (int)cn[k] : CAP - lo;
        m = j1[k] - j0[k] > m ? j1[k] - j0[k] : m;
      }
      for (int j = 0; __any(j < m); ++j) {
#pragma unroll
        for (int k = 0; k < 4; ++k) {
          const int jj = j0[k] + j;
          if (jj < j1[k]) *(LDS unsigned*)(lds + lb + ((int)ix[k] - wb + jj) * 4) = rb[k] + jj;
        }
      }
    }
    unsigned rec[8], mt[8];
#pragma unroll
    for (int m = 0; m < 8; ++m) {
      const int i = l + 64 * m;
      const unsigned v = *(LDS unsigned*)(lds + lb + i * 4);
      rec[m] = i < nw ? v : 0u;
    }
#pragma unroll
    for (int m = 0; m < 8; ++m) mt[m] = __builtin_amdgcn_raw_buffer_load_b32(sm, rec[m] * 4, 0, 0);
    if (first) { BAR(); first = false; }
#pragma unroll
    for (int m = 0; m < 8; ++m) {
      const int i = l + 64 * m;
      const unsigned v = i < nw ? mt[m] : (31u << 27);
      *(LDS unsigned*)(lds + la + i * 4) = v & 0x07FFFFFFu;
      *(LDS unsigned*)(lds + lb + i * 4) = (rec[m] << 5) | (v & 0xF8000000u);
    }
    unsigned sa[2];
    auto issue = [&](int slot, int t, const u32x4 A, const unsigned B) {
      asm volatile("s_waitcnt lgkmcnt(0)" ::: "memory");
#pragma unroll
      for (int s = 0; s < 4; ++s) dma16(ldsb + stg + slot * 5120 + s * 1024, A[s] | xch[s], sx, 0);
      dma16(ldsb + stg + slot * 5120 + 4096, (B & 0x07FFFFFFu) | ech, se, 0);
      sa[slot] = (B >> 26) ^ skey;
    };
    asm volatile("" ::: "memory");
    if (nt > 0) { const u32x4 A = *(LDS u32x4*)(lds + aoff); const unsigned B = *(LDS unsigned*)(lds + boff); issue(0, 0, A, B); }
    if (nt > 1) { const u32x4 A = *(LDS u32x4*)(lds + aoff + 128); const unsigned B = *(LDS unsigned*)(lds + boff + 128); issue(1, 1, A, B); }

    auto tile = [&](const int slot, const int t) {
      if (t + 1 < nt) asm volatile("s_waitcnt vmcnt(5)" ::: "memory"); else asm volatile("s_waitcnt vmcnt(0)" ::: "memory");
      *(LDS unsigned short*)(lds + sa[slot]) = 0x3F80;
      asm volatile("" ::: "memory");
      bf16x8 xf[4];
#pragma unroll
      for (int s = 0; s < 4; ++s) xf[s] = *(LDS bf16x8*)(lds + xrd[s] + slot * 5120);
      const bf16x8 ef = *(LDS bf16x8*)(lds + erd + slot * 5120);
      const bf16x8 sr0 = *(LDS bf16x8*)(lds + srow0), sr1 = *(LDS bf16x8*)(lds + srow1);
      s16x4 q00 = __builtin_amdgcn_ds_read_tr16_b64_v4i16((LDS s16x4*)(lds + str0));
      s16x4 q01 = __builtin_amdgcn_ds_read_tr16_b64_v4i16((LDS s16x4*)(lds + str1));
      s16x4 q10 = __builtin_amdgcn_ds_read_tr16_b64_v4i16((LDS s16x4*)(lds + str0 + 1024));
      s16x4 q11 = __builtin_amdgcn_ds_read_tr16_b64_v4i16((LDS s16x4*)(lds + str1 + 1024));
      asm volatile("" ::: "memory");
      *(LDS unsigned short*)(lds + sa[slot]) = 0;
      if (t + 2 < nt) {
        const u32x4 A = *(LDS u32x4*)(lds + aoff + (t + 2) * 128);
        const unsigned B = *(LDS unsigned*)(lds + boff + (t + 2) * 128);
        issue(slot, t + 2, A, B);
      }
      f32x16 a0, a1;
#pragma unroll
      for (int i = 0; i < 16; ++i) { a0[i] = 0.f; a1[i] = 0.f; }
      a0 = MFMA(sr0, P2[0][0], a0); a1 = MFMA(sr0, P2[0][1], a1);
      a0 = MFMA(sr1, P2[1][0], a0); a1 = MFMA(sr1, P2[1][1], a1);
#pragma unroll
      for (int s = 0; s < 4; ++s) { a0 = MFMA(xf[s], W1[s][0], a0); a1 = MFMA(xf[s], W1[s][1], a1); }
      a0 = MFMA(ef, W3[0], a0); a1 = MFMA(ef, W3[1], a1);
      float u[32];
      SB();
#pragma unroll
      for (int i = 0; i < 16; ++i) { u[i] = a0[i] * -1.4426950408889634f; }
#pragma unroll
      for (int i = 0; i < 16; ++i) { u[16 + i] = a1[i] * -1.4426950408889634f; }
      SB();
#pragma unroll
      for (int i = 0; i < 32; ++i) u[i] = __builtin_amdgcn_exp2f(u[i]);
      SB();
#pragma unroll
      for (int i = 0; i < 32; ++i) u[i] = 1.0f + u[i];
      SB();
#pragma unroll
      for (int i = 0; i < 32; ++i) u[i] = __builtin_amdgcn_rcpf(u[i]);
      SB();
#pragma unroll
      for (int i = 0; i < 16; ++i) { u[i] *= a0[i]; }
#pragma unroll
      for (int i = 0; i < 16; ++i) { u[16 + i] *= a1[i]; }
      SB();
      const bf16x8 S0 = __builtin_bit_cast(bf16x8, __builtin_shufflevector(q00, q01, 0, 1, 2, 3, 4, 5, 6, 7));
      const bf16x8 S1 = __builtin_bit_cast(bf16x8, __builtin_shufflevector(q10, q11, 0, 1, 2, 3, 4, 5, 6, 7));
      const bf16x8 m00 = __builtin_bit_cast(bf16x8, u32x4{pk(u[0], u[1]), pk(u[2], u[3]), pk(u[4], u[5]), pk(u[6], u[7])});
      const bf16x8 m10 = __builtin_bit_cast(bf16x8, u32x4{pk(u[16], u[17]), pk(u[18], u[19]), pk(u[20], u[21]), pk(u[22], u[23])});
      Y0 = MFMA(m00, S0, Y0);
      Y1 = MFMA(m10, S0, Y1);
      const bf16x8 m01 = __builtin_bit_cast(bf16x8, u32x4{pk(u[8], u[9]), pk(u[10], u[11]), pk(u[12], u[13]), pk(u[14], u[15])});
      const bf16x8 m11 = __builtin_bit_cast(bf16x8, u32x4{pk(u[24], u[25]), pk(u[26], u[27]), pk(u[28], u[29]), pk(u[30], u[31])});
      Y0 = MFMA(m01, S1, Y0);
      Y1 = MFMA(m11, S1, Y1);
    };
    for (int t = 0; t < nt; t += 2) {
      tile(0, t);
      if (t + 1 < nt) tile(1, t + 1);
    }
    wb += CAP;
  } while (wb < n);

  f32x16 o0, o1;
#pragma unroll
  for (int i = 0; i < 16; ++i) { o0[i] = 0.f; o1[i] = 0.f; }
  auto ufrag = [&](int f) { return *(LDS bf16x8*)(lds + L_WU + f * 1024 + l * 16); };
#pragma unroll
  for (int s = 0; s < 4; ++s) { o0 = MFMA(ufrag(2 * s), XO[s], o0); o1 = MFMA(ufrag(2 * s + 1), XO[s], o1); }
#pragma unroll
  for (int s = 0; s < 2; ++s) {
    const bf16x8 z0 = __builtin_bit_cast(bf16x8, u32x4{pk(Y0[8 * s], Y0[8 * s + 1]), pk(Y0[8 * s + 2], Y0[8 * s + 3]), pk(Y0[8 * s + 4], Y0[8 * s + 5]), pk(Y0[8 * s + 6], Y0[8 * s + 7])});
    const bf16x8 z1 = __builtin_bit_cast(bf16x8, u32x4{pk(Y1[8 * s], Y1[8 * s + 1]), pk(Y1[8 * s + 2], Y1[8 * s + 3]), pk(Y1[8 * s + 4], Y1[8 * s + 5]), pk(Y1[8 * s + 6], Y1[8 * s + 7])});
    o0 = MFMA(ufrag(8 + 2 * s), z0, o0); o1 = MFMA(ufrag(9 + 2 * s), z0, o1);
    o0 = MFMA(ufrag(12 + 2 * s), z1, o0); o1 = MFMA(ufrag(13 + 2 * s), z1, o1);
  }
#pragma unroll
  for (int q = 0; q < 4; ++q) {
    const u32x2 v0 = {pk(silu1(o0[4 * q]), silu1(o0[4 * q + 1])), pk(silu1(o0[4 * q + 2]), silu1(o0[4 * q + 3]))};
    const u32x2 v1 = {pk(silu1(o1[4 * q]), silu1(o1[4 * q + 1])), pk(silu1(o1[4 * q + 2]), silu1(o1[4 * q + 3]))};
    *(LDS u32x2*)(lds + stg + r * 128 + ((q ^ (r & 7)) << 4) + h * 8) = v0;
    *(LDS u32x2*)(lds + stg + r * 128 + (((4 + q) ^ (r & 7)) << 4) + h * 8) = v1;
  }
  asm volatile("" ::: "memory");
#pragma unroll
  for (int it = 0; it < 4; ++it) {
    const int m = l + 64 * it, row = m >> 3, p = m & 7;
    if (m < GP * 8) {
      const u32x4 v = *(LDS u32x4*)(lds + stg + row * 128 + ((p ^ (row & 7)) << 4));
      *(u32x4*)(out + (size_t)(GP * g) * 128 + m * 16) = v;
    }
  }
}

static inline void launch_part(void* const* d_in, void* d_ws, hipStream_t stream) {
  k_part<<<NC + 3, 1024, 0, stream>>>((const i32x4*)d_in[3], (const i32x4*)d_in[2], (const u32x4*)d_in[1], (const uint16_t*)d_in[4], (const uint16_t*)d_in[5], (char*)d_ws);
}
static inline void launch_main(void* const* d_in, void* d_out, void* d_ws, hipStream_t stream) {
  k_main<<<NB, 512, 0, stream>>>((const char*)d_in[0], (const char*)d_ws, (char*)d_out);
}

extern "C" void kernel_launch(void* const* d_in, const int* in_sizes, int n_in, void* d_out, int out_size, void* d_ws, size_t ws_size,
                              hipStream_t stream) {
  launch_part(d_in, d_ws, stream);
  launch_main(d_in, d_out, d_ws, stream);
}
```

```cpp
#include <hip/hip_runtime.h>
#include <stdint.h>
#define DIRECT_E
#define EPOL "nt"

#define LDS __attribute__((address_space(3)))
#define DI __device__ __forceinline__
typedef float f32x16 __attribute__((ext_vector_type(16)));
typedef float f32x2 __attribute__((ext_vector_type(2)));
typedef unsigned u32x4 __attribute__((ext_vector_type(4)));
typedef unsigned u32x2 __attribute__((ext_vector_type(2)));
typedef int i32x4 __attribute__((ext_vector_type(4)));
typedef __bf16 bf16x8 __attribute__((ext_vector_type(8)));
typedef __bf16 bf16x2 __attribute__((ext_vector_type(2)));
typedef short s16x4 __attribute__((ext_vector_type(4)));

constexpr int NN = 50000;
constexpr int NE = 800000;
constexpr int GP = 25;
constexpr int CE = 3200;
constexpr int NC = 250;
constexpr int NB = 250;
constexpr int CAP = 512;
constexpr unsigned WS_EP = 0, WS_META = 25600000u, WS_OFF = 28800000u, WS_WF = 29824000u;

#define MFMA(a, b, c) __builtin_amdgcn_mfma_f32_32x32x16_bf16((a), (b), (c), 0, 0, 0)
#define BAR() asm volatile("s_waitcnt lgkmcnt(0)\n\ts_barrier" ::: "memory")
#define SB() __builtin_amdgcn_sched_barrier(0)
#define STAMP(k)
#define STAMPC(k, v)

DI unsigned pk(float a, float b) { f32x2 v = {a, b}; bf16x2 r = __builtin_convertvector(v, bf16x2); return __builtin_bit_cast(unsigned, r); }
DI unsigned wave_incl_scan(unsigned x) {
  x += __builtin_amdgcn_update_dpp(0u, x, 0x111, 0xf, 0xf, false);
  x += __builtin_amdgcn_update_dpp(0u, x, 0x112, 0xf, 0xf, false);
  x += __builtin_amdgcn_update_dpp(0u, x, 0x114, 0xf, 0xf, false);
  x += __builtin_amdgcn_update_dpp(0u, x, 0x118, 0xf, 0xf, false);
  x += __builtin_amdgcn_update_dpp(0u, x, 0x142, 0xa, 0xf, false);
  x += __builtin_amdgcn_update_dpp(0u, x, 0x143, 0xc, 0xf, false);
  return x;
}

__global__ __launch_bounds__(1024) void k_part(const i32x4* __restrict__ dst4, const i32x4* __restrict__ src4, const u32x4* __restrict__ e16,
                                               const uint16_t* __restrict__ wm, const uint16_t* __restrict__ wu, char* __restrict__ ws) {
  __shared__ unsigned cnt[2048];
  __shared__ __attribute__((aligned(16))) unsigned metaL[CE];
  __shared__ __attribute__((aligned(16))) unsigned short posL[CE];
  __shared__ unsigned wsum[16];
  const int tid = threadIdx.x, c = blockIdx.x;
#undef PROFBUF
#define PROFBUF (ws + (8u << 20))
  if (c >= NC) {
    const int it = (c - NC) * 1024 + tid;
    if (it < 34 * 64) {
      const int f = it >> 6, l = it & 63, col = l & 31, h = l >> 5;
      unsigned short v[8];
#pragma unroll
      for (int j = 0; j < 8; ++j) {
        if (f < 18) {
          const int t = f & 1, s = (f >> 1) & 3;
          const int row = (f < 8 ? 0 : (f < 16 ? 64 : 128)) + (f < 16 ? 16 * s : 0) + 8 * h + j;
          v[j] = wm[row * 64 + 32 * t + col];
        } else {
          const int ks = (f - 18) >> 1, u = f & 1;
          const int row = ks < 4 ? 16 * ks + 8 * h + j : 64 + 32 * ((ks - 4) >> 1) + 16 * (ks & 1) + 8 * (j >> 2) + 4 * h + (j & 3);
          v[j] = wu[row * 64 + 32 * u + col];
        }
      }
      u32x4 o = {v[0] | ((unsigned)v[1] << 16), v[2] | ((unsigned)v[3] << 16), v[4] | ((unsigned)v[5] << 16), v[6] | ((unsigned)v[7] << 16)};
      *(u32x4*)(ws + WS_WF + it * 16) = o;
    }
    return;
  }
  STAMP(0);
  i32x4 d = {0, 0, 0, 0}, s = {0, 0, 0, 0};
  if (tid < CE / 4) { d = dst4[c * (CE / 4) + tid]; s = src4[c * (CE / 4) + tid]; }
  cnt[tid] = 0; cnt[tid + 1024] = 0;
  BAR();
  STAMP(1);
  unsigned g[4], rk[4];
  if (tid < CE / 4) {
#pragma unroll
    for (int j = 0; j < 4; ++j) { g[j] = (unsigned)d[j] / (unsigned)GP; rk[j] = __hip_atomic_fetch_add(&cnt[g[j]], 1u, __ATOMIC_RELAXED, __HIP_MEMORY_SCOPE_WORKGROUP); }
  }
  BAR();
  STAMP(2);
  const u32x2 cc = *(const u32x2*)&cnt[2 * tid];
  const unsigned incl = wave_incl_scan(cc[0] + cc[1]);
  if ((tid & 63) == 63) wsum[tid >> 6] = incl;
  BAR();
  unsigned ws16 = wsum[tid & 15];
  ws16 += __builtin_amdgcn_update_dpp(0u, ws16, 0x111, 0xf, 0xf, false);
  ws16 += __builtin_amdgcn_update_dpp(0u, ws16, 0x112, 0xf, 0xf, false);
  ws16 += __builtin_amdgcn_update_dpp(0u, ws16, 0x114, 0xf, 0xf, false);
  ws16 += __builtin_amdgcn_update_dpp(0u, ws16, 0x118, 0xf, 0xf, false);
  const int w = __builtin_amdgcn_readfirstlane(tid >> 6);
  const unsigned wb = w ? __builtin_amdgcn_readlane(ws16, w - 1) : 0u;
  const unsigned o0 = wb + incl - cc[0] - cc[1], o1 = o0 + cc[0];
  *(u32x2*)&cnt[2 * tid] = u32x2{o0, o1};
  *(unsigned*)(ws + WS_OFF + c * 4096 + tid * 4) = o0 | (o1 << 16);
  BAR();
  STAMP(3);
  if (tid < CE / 4) {
    unsigned p[4];
#pragma unroll
    for (int j = 0; j < 4; ++j) {
      p[j] = cnt[g[j]] + rk[j];
      metaL[p[j]] = ((unsigned)s[j] * GP + ((unsigned)d[j] - g[j] * GP)) * CE + (4 * tid + j);
    }
  }
  BAR();
  STAMP(4);
  if (tid < CE / 4) *(u32x4*)(ws + WS_META + c * (CE * 4) + tid * 16) = *(const u32x4*)&metaL[4 * tid];
  STAMP(5);
  STAMP(6);
}

constexpr unsigned L_STG = 0;
constexpr unsigned L_S = 81920;
constexpr unsigned L_LIST = 98304;
constexpr unsigned L_WU = 131072;
constexpr unsigned L_T1 = 147456;
constexpr unsigned L_T2 = 151552;
constexpr unsigned L_END = 152064;

DI void dma16(unsigned m0, unsigned voff, __amdgpu_buffer_rsrc_t srd, unsigned soff) {
  asm volatile("s_mov_b32 m0, %0\n\ts_nop 0\n\tbuffer_load_dwordx4 %1, %2, %3 offen lds" ::"s"(m0), "v"(voff), "s"(srd), "s"(soff) : "m0", "memory");
}
DI float silu1(float v) { return v * __builtin_amdgcn_rcpf(1.0f + __builtin_amdgcn_exp2f(v * -1.4426950408889634f)); }

__global__ __launch_bounds__(512, 2) void k_main(const char* __restrict__ x, const char* __restrict__ ea, const char* __restrict__ ws, char* __restrict__ out) {
  __shared__ __attribute__((aligned(1024))) char lds_[L_END];
  LDS char* lds = (LDS char*)lds_;
  const unsigned ldsb = (unsigned)(size_t)lds;
  const int tid = threadIdx.x, l = tid & 63, w = __builtin_amdgcn_readfirstlane(tid >> 6);
  const int xcd = blockIdx.x & 7, b = xcd * 31 + (xcd < 2 ? xcd : 2) + (blockIdx.x >> 3);
  const int g = 8 * b + w;
  const int r = l & 31, h = l >> 5;
#undef PROFBUF
#define PROFBUF ((char*)ws)
  STAMP(0);
  const auto sx = __builtin_amdgcn_make_buffer_rsrc((void*)x, 0, NN * 128, 0x00020000);
  const auto se = __builtin_amdgcn_make_buffer_rsrc((void*)ea, 0, NE * 32, 0x00020000);
  const auto sm = __builtin_amdgcn_make_buffer_rsrc((void*)(ws + WS_META), 0, NE * 4, 0x00020000);
  const auto sw = __builtin_amdgcn_make_buffer_rsrc((void*)(ws + WS_WF), 0, 34 * 1024, 0x00020000);

  u32x4 t1 = {0, 0, 0, 0}; unsigned short t2 = 0;
  if (tid < NC) { const char* row = ws + WS_OFF + tid * 4096 + b * 16; t1 = *(const u32x4*)row; t2 = *(const unsigned short*)(row + 16); }
  const unsigned stg = L_STG + w * 10240;
  asm volatile("s_nop 4" ::: "memory");
  for (int f = w; f < 34; f += 8) {
    const unsigned dstl = f < 18 ? L_STG + (f / 5) * 10240 + 5120 + (f % 5) * 1024 : L_WU + (f - 18) * 1024;
    dma16(ldsb + dstl, l * 16, sw, f * 1024);
  }
#pragma unroll
  for (int s = 0; s < 4; ++s) {
    const int R = 8 * s + (l >> 3);
    int node = GP * g + R; node = node < NN ? node : NN - 1;
    dma16(ldsb + stg + s * 1024, node * 128 + (((l & 7) ^ ((R >> 1) & 7)) << 4), sx, 0);
  }
  const unsigned sreg = L_S + w * 2048;
  *(LDS u32x4*)(lds + sreg + l * 16) = u32x4{0, 0, 0, 0};
  *(LDS u32x4*)(lds + sreg + 1024 + l * 16) = u32x4{0, 0, 0, 0};
  if (tid < NC) { *(LDS u32x4*)(lds + L_T1 + tid * 16) = t1; *(LDS unsigned short*)(lds + L_T2 + tid * 2) = t2; }
  asm volatile("s_waitcnt vmcnt(0)" ::: "memory");
  BAR();
  STAMP(1);

  bf16x8 W1[4][2], W3[2], P2[2][2], XO[4];
  {
    auto wfrag = [&](int f) { return *(LDS bf16x8*)(lds + L_STG + (f / 5) * 10240 + 5120 + (f % 5) * 1024 + l * 16); };
#pragma unroll
    for (int s = 0; s < 4; ++s) { W1[s][0] = wfrag(2 * s); W1[s][1] = wfrag(2 * s + 1); }
    W3[0] = wfrag(16); W3[1] = wfrag(17);
    const int rs = (r & 19) | ((r & 4) << 1) | ((r & 8) >> 1);
    f32x16 p0, p1;
#pragma unroll
    for (int i = 0; i < 16; ++i) { p0[i] = 0.f; p1[i] = 0.f; }
#pragma unroll
    for (int s = 0; s < 4; ++s) {
      const bf16x8 a = *(LDS bf16x8*)(lds + stg + rs * 128 + (((2 * s + h) ^ ((rs >> 1) & 7)) << 4));
      p0 = MFMA(a, wfrag(8 + 2 * s), p0);
      p1 = MFMA(a, wfrag(9 + 2 * s), p1);
      XO[s] = *(LDS bf16x8*)(lds + stg + r * 128 + (((2 * s + h) ^ ((r >> 1) & 7)) << 4));
    }
#pragma unroll
    for (int s = 0; s < 2; ++s) {
      P2[s][0] = __builtin_bit_cast(bf16x8, u32x4{pk(p0[8 * s], p0[8 * s + 1]), pk(p0[8 * s + 2], p0[8 * s + 3]), pk(p0[8 * s + 4], p0[8 * s + 5]), pk(p0[8 * s + 6], p0[8 * s + 7])});
      P2[s][1] = __builtin_bit_cast(bf16x8, u32x4{pk(p1[8 * s], p1[8 * s + 1]), pk(p1[8 * s + 2], p1[8 * s + 3]), pk(p1[8 * s + 4], p1[8 * s + 5]), pk(p1[8 * s + 6], p1[8 * s + 7])});
    }
  }
  f32x16 Y0, Y1;
#pragma unroll
  for (int i = 0; i < 16; ++i) { Y0[i] = 0.f; Y1[i] = 0.f; }

  const unsigned la = L_LIST + w * 4096, lb = la + 2048;
  unsigned xch[4];
#pragma unroll
  for (int s = 0; s < 4; ++s) xch[s] = ((l & 7) ^ (((8 * s + (l >> 3)) >> 1) & 7)) << 4;
  const unsigned ech = ((l & 1) ^ ((l >> 4) & 1)) << 4;
  const unsigned skey = sreg + (l >> 1) * 64 + (((l >> 3) & 3) << 4);
  const unsigned aoff = la + (l >> 3) * 16;
  const unsigned boff = lb + (4 * ((l >> 1) & 7) + (l >> 4)) * 4;
  unsigned xrd[4];
#pragma unroll
  for (int s = 0; s < 4; ++s) xrd[s] = stg + r * 128 + (((2 * s + h) ^ ((r >> 1) & 7)) << 4);
  const unsigned erd = stg + 4096 + r * 32 + ((h ^ ((r >> 3) & 1)) << 4);
  const unsigned srow0 = sreg + r * 64 + ((h ^ ((r >> 2) & 3)) << 4), srow1 = srow0 ^ 32;
  const unsigned str0 = sreg + (4 * h + ((l & 15) >> 2)) * 64 + (((2 * ((l >> 4) & 1) + ((l & 3) >> 1)) ^ h) << 4) + (l & 1) * 8;
  const unsigned str1 = (str0 ^ 32) + 512;

  STAMP(2);
  int wb = 0, n;
  bool first = true;
  do {
    unsigned cn[4], rb[4], ix[4];
    unsigned tot = 0;
#pragma unroll
    for (int k = 0; k < 4; ++k) {
      const int c = l + 64 * k;
      const int cc = c < NC ? c : 0;
      const unsigned o0 = *(LDS unsigned short*)(lds + L_T1 + cc * 16 + w * 2);
      const unsigned o1 = *(LDS unsigned short*)(lds + (w < 7 ? L_T1 + cc * 16 + w * 2 + 2 : L_T2 + cc * 2));
      cn[k] = c < NC ? o1 - o0 : 0u;
      rb[k] = cc * CE + o0;
      ix[k] = tot;
      tot += cn[k];
    }
    const unsigned incl = wave_incl_scan(tot);
    n = __builtin_amdgcn_readlane(incl, 63);
#pragma unroll
    for (int k = 0; k < 4; ++k) ix[k] += incl - tot;
    const int nw = n - wb < CAP ? n - wb : CAP;
    const int nt = (nw + 31) >> 5;
    {
      int j0[4], j1[4], m = 0;
#pragma unroll
      for (int k = 0; k < 4; ++k) {
        const int lo = (int)ix[k] - wb;
        j0[k] = lo < 0 ? -lo : 0;
        j1[k] = (int)cn[k] < CAP - lo ? (int)cn[k] : CAP - lo;
        m = j1[k] - j0[k] > m ? j1[k] - j0[k] : m;
      }
      for (int j = 0; __any(j < m); ++j) {
#pragma unroll
        for (int k = 0; k < 4; ++k) {
          const int jj = j0[k] + j;
          if (jj < j1[k]) *(LDS unsigned*)(lds + lb + ((int)ix[k] - wb + jj) * 4) = rb[k] + jj;
        }
      }
    }
    unsigned rec[8], mt[8];
#pragma unroll
    for (int m = 0; m < 8; ++m) {
      const int i = l + 64 * m;
      const unsigned v = *(LDS unsigned*)(lds + lb + i * 4);
      rec[m] = i < nw ? v : 0u;
    }
#pragma unroll
    for (int m = 0; m < 8; ++m) mt[m] = __builtin_amdgcn_raw_buffer_load_b32(sm, rec[m] * 4, 0, 0);
    STAMP(3);
    if (first) { BAR(); first = false; }
#pragma unroll
    for (int m = 0; m < 8; ++m) {
      const int i = l + 64 * m;
      const unsigned q = mt[m] / (unsigned)CE, idx = mt[m] - q * CE, sn = q / (unsigned)GP, dl = q - sn * GP;
      const unsigned eid = rec[m] / (unsigned)CE * CE + idx;
      *(LDS unsigned*)(lds + la + i * 4) = i < nw ? sn << 7 : 0u;
      *(LDS unsigned*)(lds + lb + i * 4) = i < nw ? (eid << 5) | (dl << 27) : (31u << 27);
    }
    STAMP(4); STAMPC(0, __builtin_amdgcn_s_memtime()); STAMPC(2, nt);
    auto dma_tile = [&](const int slot, const int tt, const u32x4 A, const unsigned B) {
      const unsigned v0 = A[0] | xch[0], v1 = A[1] | xch[1], v2 = A[2] | xch[2], v3 = A[3] | xch[3], v4 = (B & 0x07FFFFFFu) | ech;
      asm volatile("s_waitcnt lgkmcnt(0)\n\t"
                   "s_cmp_lt_i32 %[t], %[nt]\n\ts_cbranch_scc0 1f\n\t"
                   "s_mov_b32 m0, %[m]\n\ts_nop 0\n\tbuffer_load_dwordx4 %[v0], %[sx], 0 offen lds\n\t"
                   "s_add_u32 m0, %[m], 0x400\n\ts_nop 0\n\tbuffer_load_dwordx4 %[v1], %[sx], 0 offen lds\n\t"
                   "s_add_u32 m0, %[m], 0x800\n\ts_nop 0\n\tbuffer_load_dwordx4 %[v2], %[sx], 0 offen lds\n\t"
                   "s_add_u32 m0, %[m], 0xc00\n\ts_nop 0\n\tbuffer_load_dwordx4 %[v3], %[sx], 0 offen lds\n\t"
                   "s_add_u32 m0, %[m], 0x1000\n\ts_nop 0\n\tbuffer_load_dwordx4 %[v4], %[se], 0 offen " EPOL " lds\n\t"
                   "1:"
                   ::[t] "s"(tt), [nt] "s"(nt), [m] "s"(ldsb + stg + slot * 5120), [v0] "v"(v0), [v1] "v"(v1), [v2] "v"(v2), [v3] "v"(v3), [v4] "v"(v4), [sx] "s"(sx), [se] "s"(se)
                   : "m0", "scc", "memory");
    };
    auto wait_dma = [&](const int tt) {
      asm volatile("s_cmp_lt_i32 %0, %1\n\ts_cbranch_scc1 1f\n\ts_waitcnt vmcnt(0)\n\t1:\n\ts_waitcnt vmcnt(5)" ::"s"(tt), "s"(nt) : "scc", "memory");
    };
    f32x16 a0, a1;
    bf16x8 xf[4], ef, sr0, sr1;
    s16x4 q00 = {0, 0, 0, 0}, q01 = q00, q10 = q00, q11 = q00;
    u32x4 mm0 = {0, 0, 0, 0}, mm1 = mm0, A4;
    unsigned Bq[2], sa;
    f32x16 zero;
#pragma unroll
    for (int i = 0; i < 16; ++i) zero[i] = 0.f;
    auto rd_frags = [&](const int slot) {
#pragma unroll
      for (int s = 0; s < 4; ++s) xf[s] = *(LDS bf16x8*)(lds + xrd[s] + slot * 5120);
      ef = *(LDS bf16x8*)(lds + erd + slot * 5120);
      sr0 = *(LDS bf16x8*)(lds + srow0); sr1 = *(LDS bf16x8*)(lds + srow1);
    };
    asm volatile("" ::: "memory");
    {
      A4 = *(LDS u32x4*)(lds + aoff); Bq[0] = *(LDS unsigned*)(lds + boff);
      const u32x4 A1 = *(LDS u32x4*)(lds + aoff + 128); Bq[1] = *(LDS unsigned*)(lds + boff + 128);
      dma_tile(0, 0, A4, Bq[0]);
      dma_tile(1, 1, A1, Bq[1]);
      wait_dma(1);
      sa = (Bq[0] >> 26) ^ skey;
      *(LDS unsigned short*)(lds + sa) = 0x3F80;
      asm volatile("" ::: "memory");
      rd_frags(0);
      A4 = *(LDS u32x4*)(lds + aoff + 256); Bq[0] = *(LDS unsigned*)(lds + boff + 256);
      dma_tile(0, 2, A4, Bq[0]);
      a0 = MFMA(sr0, P2[0][0], zero); a0 = MFMA(sr1, P2[1][0], a0);
#pragma unroll
      for (int s = 0; s < 4; ++s) a0 = MFMA(xf[s], W1[s][0], a0);
      a0 = MFMA(ef, W3[0], a0);
    }
#define S0_ __builtin_bit_cast(bf16x8, __builtin_shufflevector(q00, q01, 0, 1, 2, 3, 4, 5, 6, 7))
#define S1_ __builtin_bit_cast(bf16x8, __builtin_shufflevector(q10, q11, 0, 1, 2, 3, 4, 5, 6, 7))
#define BF(v) __builtin_bit_cast(bf16x8, v)
#define PIN(lo, hi) _Pragma("unroll") for (int i = lo; i < hi; i += 4) asm volatile("" : "+v"(u[i]), "+v"(u[i + 1]), "+v"(u[i + 2]), "+v"(u[i + 3])); SB();
#define MULS(lo, hi) _Pragma("unroll") for (int i = lo; i < hi; ++i) u[i] = c[i] * -1.4426950408889634f; PIN(lo, hi)
#define EXPS(lo, hi) _Pragma("unroll") for (int i = lo; i < hi; ++i) u[i] = __builtin_amdgcn_exp2f(u[i]); PIN(lo, hi)
#define ADDS(lo, hi) _Pragma("unroll") for (int i = lo; i < hi; ++i) u[i] = 1.0f + u[i]; PIN(lo, hi)
#define RCPS(lo, hi) _Pragma("unroll") for (int i = lo; i < hi; ++i) u[i] = __builtin_amdgcn_rcpf(u[i]); PIN(lo, hi)
#define FINS(lo, hi) _Pragma("unroll") for (int i = lo; i < hi; ++i) u[i] *= c[i]; PIN(lo, hi)
    auto phase = [&](const int T, const int par, const int t, f32x16& c, f32x16& nx, f32x16& Yc, f32x16& Yp) {
      float u[16];
      SB();
      MULS(0, 8)
      Yp = MFMA(BF(mm1), S1_, Yp); SB();
      MULS(8, 16)
      if (T == 0) {
        q00 = __builtin_amdgcn_ds_read_tr16_b64_v4i16((LDS s16x4*)(lds + str0));
        q01 = __builtin_amdgcn_ds_read_tr16_b64_v4i16((LDS s16x4*)(lds + str1));
        q10 = __builtin_amdgcn_ds_read_tr16_b64_v4i16((LDS s16x4*)(lds + str0 + 1024));
        q11 = __builtin_amdgcn_ds_read_tr16_b64_v4i16((LDS s16x4*)(lds + str1 + 1024));
        asm volatile("" ::: "memory");
        *(LDS unsigned short*)(lds + sa) = 0;
        sa = (Bq[par ^ 1] >> 26) ^ skey;
        SB();
      } else {
        dma_tile(par ^ 1, t + 3, A4, Bq[par ^ 1]);
        SB();
      }
      const int U = T ^ 1;
      nx = MFMA(sr0, P2[0][U], zero); SB();
      EXPS(0, 4)
      nx = MFMA(sr1, P2[1][U], nx); SB();
      EXPS(4, 8)
      nx = MFMA(xf[0], W1[0][U], nx); SB();
      EXPS(8, 12)
      nx = MFMA(xf[1], W1[1][U], nx); SB();
      EXPS(12, 16)
      nx = MFMA(xf[2], W1[2][U], nx); SB();
      ADDS(0, 16)
      nx = MFMA(xf[3], W1[3][U], nx); SB();
      RCPS(0, 4)
      nx = MFMA(ef, W3[U], nx); SB();
      if (T == 0) {
        wait_dma(t + 2);
        *(LDS unsigned short*)(lds + sa) = 0x3F80;
        asm volatile("" ::: "memory");
        rd_frags(par ^ 1);
        A4 = *(LDS u32x4*)(lds + aoff + (t + 3) * 128);
        Bq[par ^ 1] = *(LDS unsigned*)(lds + boff + (t + 3) * 128);
        SB();
      }
      RCPS(4, 16)
      FINS(0, 16)
      mm0 = u32x4{pk(u[0], u[1]), pk(u[2], u[3]), pk(u[4], u[5]), pk(u[6], u[7])}; SB();
      mm1 = u32x4{pk(u[8], u[9]), pk(u[10], u[11]), pk(u[12], u[13]), pk(u[14], u[15])}; SB();
      Yc = MFMA(BF(mm0), S0_, Yc); SB();
    };
    for (int t = 0; t < nt; t += 2) {
      phase(0, 0, t, a0, a1, Y0, Y1);
      phase(1, 0, t, a1, a0, Y1, Y0);
      if (t + 1 >= nt) break;
      phase(0, 1, t + 1, a0, a1, Y0, Y1);
      phase(1, 1, t + 1, a1, a0, Y1, Y0);
    }
    Y1 = MFMA(BF(mm1), S1_, Y1);
    *(LDS unsigned short*)(lds + sa) = 0;
    wb += CAP;
  } while (wb < n);

  STAMP(5); STAMPC(1, __builtin_amdgcn_s_memtime());
  f32x16 o0, o1;
#pragma unroll
  for (int i = 0; i < 16; ++i) { o0[i] = 0.f; o1[i] = 0.f; }
  auto ufrag = [&](int f) { return *(LDS bf16x8*)(lds + L_WU + f * 1024 + l * 16); };
#pragma unroll
  for (int s = 0; s < 4; ++s) { o0 = MFMA(ufrag(2 * s), XO[s], o0); o1 = MFMA(ufrag(2 * s + 1), XO[s], o1); }
#pragma unroll
  for (int s = 0; s < 2; ++s) {
    const bf16x8 z0 = __builtin_bit_cast(bf16x8, u32x4{pk(Y0[8 * s], Y0[8 * s + 1]), pk(Y0[8 * s + 2], Y0[8 * s + 3]), pk(Y0[8 * s + 4], Y0[8 * s + 5]), pk(Y0[8 * s + 6], Y0[8 * s + 7])});
    const bf16x8 z1 = __builtin_bit_cast(bf16x8, u32x4{pk(Y1[8 * s], Y1[8 * s + 1]), pk(Y1[8 * s + 2], Y1[8 * s + 3]), pk(Y1[8 * s + 4], Y1[8 * s + 5]), pk(Y1[8 * s + 6], Y1[8 * s + 7])});
    o0 = MFMA(ufrag(8 + 2 * s), z0, o0); o1 = MFMA(ufrag(9 + 2 * s), z0, o1);
    o0 = MFMA(ufrag(12 + 2 * s), z1, o0); o1 = MFMA(ufrag(13 + 2 * s), z1, o1);
  }
#pragma unroll
  for (int q = 0; q < 4; ++q) {
    const u32x2 v0 = {pk(silu1(o0[4 * q]), silu1(o0[4 * q + 1])), pk(silu1(o0[4 * q + 2]), silu1(o0[4 * q + 3]))};
    const u32x2 v1 = {pk(silu1(o1[4 * q]), silu1(o1[4 * q + 1])), pk(silu1(o1[4 * q + 2]), silu1(o1[4 * q + 3]))};
    *(LDS u32x2*)(lds + stg + r * 128 + ((q ^ (r & 7)) << 4) + h * 8) = v0;
    *(LDS u32x2*)(lds + stg + r * 128 + (((4 + q) ^ (r & 7)) << 4) + h * 8) = v1;
  }
  asm volatile("" ::: "memory");
#pragma unroll
  for (int it = 0; it < 4; ++it) {
    const int m = l + 64 * it, row = m >> 3, p = m & 7;
    if (m < GP * 8) {
      const u32x4 v = *(LDS u32x4*)(lds + stg + row * 128 + ((p ^ (row & 7)) << 4));
      *(u32x4*)(out + (size_t)(GP * g) * 128 + m * 16) = v;
    }
  }
  STAMP(6);
}

static inline void launch_part(void* const* d_in, void* d_ws, hipStream_t stream) {
  k_part<<<NC + 3, 1024, 0, stream>>>((const i32x4*)d_in[3], (const i32x4*)d_in[2], (const u32x4*)d_in[1], (const uint16_t*)d_in[4], (const uint16_t*)d_in[5], (char*)d_ws);
}
static inline void launch_main(void* const* d_in, void* d_out, void* d_ws, hipStream_t stream) {
  k_main<<<NB, 512, 0, stream>>>((const char*)d_in[0], (const char*)d_in[1], (const char*)d_ws, (char*)d_out);
}

extern "C" void kernel_launch(void* const* d_in, const int* in_sizes, int n_in, void* d_out, int out_size, void* d_ws, size_t ws_size,
                              hipStream_t stream) {
  launch_part(d_in, d_ws, stream);
  launch_main(d_in, d_out, d_ws, stream);
}
```

```cpp
#include <hip/hip_runtime.h>
#include <stdint.h>

#define LDS __attribute__((address_space(3)))
#define DI __device__ __forceinline__
typedef float f32x16 __attribute__((ext_vector_type(16)));
typedef float f32x2 __attribute__((ext_vector_type(2)));
typedef unsigned u32x4 __attribute__((ext_vector_type(4)));
typedef unsigned u32x2 __attribute__((ext_vector_type(2)));
typedef int i32x4 __attribute__((ext_vector_type(4)));
typedef __bf16 bf16x8 __attribute__((ext_vector_type(8)));
typedef __bf16 bf16x2 __attribute__((ext_vector_type(2)));
typedef short s16x4 __attribute__((ext_vector_type(4)));

constexpr int NN = 50000;
constexpr int NE = 800000;
constexpr int GP = 25;
constexpr int CE = 3200;
constexpr int NC = 250;
constexpr int NB = 250;
constexpr int CAP = 512;
constexpr unsigned WS_EP = 0, WS_META = 25600000u, WS_OFF = 28800000u, WS_WF = 29824000u;

#define MFMA(a, b, c) __builtin_amdgcn_mfma_f32_32x32x16_bf16((a), (b), (c), 0, 0, 0)
#define BAR() asm volatile("s_waitcnt lgkmcnt(0)\n\ts_barrier" ::: "memory")
#define SB() __builtin_amdgcn_sched_barrier(0)
#define EPOL ""
#define STAMP(k)
#define STAMPC(k, v)

DI unsigned pk(float a, float b) { f32x2 v = {a, b}; bf16x2 r = __builtin_convertvector(v, bf16x2); return __builtin_bit_cast(unsigned, r); }
DI unsigned wave_incl_scan(unsigned x) {
  x += __builtin_amdgcn_update_dpp(0u, x, 0x111, 0xf, 0xf, false);
  x += __builtin_amdgcn_update_dpp(0u, x, 0x112, 0xf, 0xf, false);
  x += __builtin_amdgcn_update_dpp(0u, x, 0x114, 0xf, 0xf, false);
  x += __builtin_amdgcn_update_dpp(0u, x, 0x118, 0xf, 0xf, false);
  x += __builtin_amdgcn_update_dpp(0u, x, 0x142, 0xa, 0xf, false);
  x += __builtin_amdgcn_update_dpp(0u, x, 0x143, 0xc, 0xf, false);
  return x;
}

__global__ __launch_bounds__(1024) void k_part(const i32x4* __restrict__ dst4, const i32x4* __restrict__ src4, const u32x4* __restrict__ e16,
                                               const uint16_t* __restrict__ wm, const uint16_t* __restrict__ wu, char* __restrict__ ws) {
  __shared__ unsigned cnt[2048];
  __shared__ __attribute__((aligned(16))) unsigned metaL[CE];
  __shared__ __attribute__((aligned(16))) unsigned short posL[CE];
  __shared__ unsigned wsum[16];
  const int tid = threadIdx.x, c = blockIdx.x;
#undef PROFBUF
#define PROFBUF (ws + (8u << 20))
  if (c >= NC) {
    const int it = (c - NC) * 1024 + tid;
    if (it < 34 * 64) {
      const int f = it >> 6, l = it & 63, col = l & 31, h = l >> 5;
      unsigned short v[8];
#pragma unroll
      for (int j = 0; j < 8; ++j) {
        if (f < 18) {
          const int t = f & 1, s = (f >> 1) & 3;
          const int row = (f < 8 ? 0 : (f < 16 ? 64 : 128)) + (f < 16 ? 16 * s : 0) + 8 * h + j;
          v[j] = wm[row * 64 + 32 * t + col];
        } else {
          const int ks = (f - 18) >> 1, u = f & 1;
          const int row = ks < 4 ? 16 * ks + 8 * h + j : 64 + 32 * ((ks - 4) >> 1) + 16 * (ks & 1) + 8 * (j >> 2) + 4 * h + (j & 3);
          v[j] = wu[row * 64 + 32 * u + col];
        }
      }
      u32x4 o = {v[0] | ((unsigned)v[1] << 16), v[2] | ((unsigned)v[3] << 16), v[4] | ((unsigned)v[5] << 16), v[6] | ((unsigned)v[7] << 16)};
      *(u32x4*)(ws + WS_WF + it * 16) = o;
    }
    return;
  }
  STAMP(0);
  i32x4 d = {0, 0, 0, 0}, s = {0, 0, 0, 0};
  if (tid < CE / 4) { d = dst4[c * (CE / 4) + tid]; s = src4[c * (CE / 4) + tid]; }
  cnt[tid] = 0; cnt[tid + 1024] = 0;
  BAR();
  STAMP(1);
  unsigned g[4], rk[4];
  if (tid < CE / 4) {
#pragma unroll
    for (int j = 0; j < 4; ++j) { g[j] = (unsigned)d[j] / (unsigned)GP; rk[j] = __hip_atomic_fetch_add(&cnt[g[j]], 1u, __ATOMIC_RELAXED, __HIP_MEMORY_SCOPE_WORKGROUP); }
  }
  BAR();
  STAMP(2);
  const u32x2 cc = *(const u32x2*)&cnt[2 * tid];
  const unsigned incl = wave_incl_scan(cc[0] + cc[1]);
  if ((tid & 63) == 63) wsum[tid >> 6] = incl;
  BAR();
  unsigned ws16 = wsum[tid & 15];
  ws16 += __builtin_amdgcn_update_dpp(0u, ws16, 0x111, 0xf, 0xf, false);
  ws16 += __builtin_amdgcn_update_dpp(0u, ws16, 0x112, 0xf, 0xf, false);
  ws16 += __builtin_amdgcn_update_dpp(0u, ws16, 0x114, 0xf, 0xf, false);
  ws16 += __builtin_amdgcn_update_dpp(0u, ws16, 0x118, 0xf, 0xf, false);
  const int w = __builtin_amdgcn_readfirstlane(tid >> 6);
  const unsigned wb = w ? __builtin_amdgcn_readlane(ws16, w - 1) : 0u;
  const unsigned o0 = wb + incl - cc[0] - cc[1], o1 = o0 + cc[0];
  *(u32x2*)&cnt[2 * tid] = u32x2{o0, o1};
  *(unsigned*)(ws + WS_OFF + c * 4096 + tid * 4) = o0 | (o1 << 16);
  BAR();
  STAMP(3);
  if (tid < CE / 4) {
    unsigned p[4];
#pragma unroll
    for (int j = 0; j < 4; ++j) {
      p[j] = cnt[g[j]] + rk[j];
      metaL[p[j]] = (unsigned)s[j] * (GP * CE) + ((unsigned)d[j] - g[j] * GP) * CE + (4 * tid + j);
    }
  }
  BAR();
  STAMP(4);
  if (tid < CE / 4) *(u32x4*)(ws + WS_META + c * (CE * 4) + tid * 16) = *(const u32x4*)&metaL[4 * tid];
  STAMP(5);
  STAMP(6);
}

constexpr unsigned L_STG = 0;
constexpr unsigned L_S = 81920;
constexpr unsigned L_LIST = 98304;
constexpr unsigned L_WU = 131072;
constexpr unsigned L_T1 = 147456;
constexpr unsigned L_T2 = 151552;
constexpr unsigned L_END = 152576;

DI void dma16(unsigned m0, unsigned voff, __amdgpu_buffer_rsrc_t srd, unsigned soff) {
  asm volatile("s_mov_b32 m0, %0\n\ts_nop 0\n\tbuffer_load_dwordx4 %1, %2, %3 offen lds" ::"s"(m0), "v"(voff), "s"(srd), "s"(soff) : "m0", "memory");
}
DI void dma4(unsigned m0, unsigned voff, __amdgpu_buffer_rsrc_t srd, unsigned soff) {
  asm volatile("s_mov_b32 m0, %0\n\ts_nop 0\n\tbuffer_load_dword %1, %2, %3 offen lds" ::"s"(m0), "v"(voff), "s"(srd), "s"(soff) : "m0", "memory");
}
DI unsigned umax(unsigned a, unsigned b) { return a > b ? a : b; }
DI unsigned wave_excl_max(unsigned x) {
  x = umax(x, __builtin_amdgcn_update_dpp(0u, x, 0x111, 0xf, 0xf, false));
  x = umax(x, __builtin_amdgcn_update_dpp(0u, x, 0x112, 0xf, 0xf, false));
  x = umax(x, __builtin_amdgcn_update_dpp(0u, x, 0x114, 0xf, 0xf, false));
  x = umax(x, __builtin_amdgcn_update_dpp(0u, x, 0x118, 0xf, 0xf, false));
  x = umax(x, __builtin_amdgcn_update_dpp(0u, x, 0x142, 0xa, 0xf, false));
  x = umax(x, __builtin_amdgcn_update_dpp(0u, x, 0x143, 0xc, 0xf, false));
  return __builtin_amdgcn_update_dpp(0u, x, 0x138, 0xf, 0xf, false);
}
DI float silu1(float v) { return v * __builtin_amdgcn_rcpf(1.0f + __builtin_amdgcn_exp2f(v * -1.4426950408889634f)); }

__global__ __launch_bounds__(512, 2) void k_main(const char* __restrict__ x, const char* __restrict__ ea, const char* __restrict__ ws, char* __restrict__ out) {
  __shared__ __attribute__((aligned(1024))) char lds_[L_END];
  LDS char* lds = (LDS char*)lds_;
  const unsigned ldsb = (unsigned)(size_t)lds;
  const int tid = threadIdx.x, l = tid & 63, w = __builtin_amdgcn_readfirstlane(tid >> 6);
  const int xcd = blockIdx.x & 7, b = xcd * 31 + (xcd < 2 ? xcd : 2) + (blockIdx.x >> 3);
  const int g = 8 * b + w;
  const int r = l & 31, h = l >> 5;
#undef PROFBUF
#define PROFBUF ((char*)ws)
  STAMP(0);
  const auto sx = __builtin_amdgcn_make_buffer_rsrc((void*)x, 0, NN * 128, 0x00020000);
  const auto se = __builtin_amdgcn_make_buffer_rsrc((void*)ea, 0, NE * 32, 0x00020000);
  const auto sm = __builtin_amdgcn_make_buffer_rsrc((void*)(ws + WS_META), 0, NE * 4, 0x00020000);
  const auto sw = __builtin_amdgcn_make_buffer_rsrc((void*)(ws + WS_WF), 0, 34 * 1024, 0x00020000);

  const auto st = __builtin_amdgcn_make_buffer_rsrc((void*)(ws + WS_OFF), 0, NC * 4096, 0x00020000);

  const unsigned stg = L_STG + w * 10240;
  asm volatile("s_nop 4" ::: "memory");
  if (w < 4) dma16(ldsb + L_T1 + w * 1024, tid * 4096 + b * 16, st, 0);
  else dma4(ldsb + L_T2 + (w - 4) * 256, (tid - 256) * 4096 + b * 16 + 16, st, 0);
#pragma unroll
  for (int i = 0; i < 5; ++i) {
    const int f = w + 8 * i;
    const unsigned dstl = f < 18 ? L_STG + (f / 5) * 10240 + 5120 + (f % 5) * 1024 : L_WU + (f - 18) * 1024;
    if (f < 34) dma16(ldsb + dstl, l * 16, sw, f * 1024);
  }
#pragma unroll
  for (int s = 0; s < 4; ++s) {
    const int R = 8 * s + (l >> 3);
    int node = GP * g + R; node = node < NN ? node : NN - 1;
    dma16(ldsb + stg + s * 1024, node * 128 + (((l & 7) ^ ((R >> 1) & 7)) << 4), sx, 0);
  }
  const unsigned sreg = L_S + w * 2048;
  const unsigned la = L_LIST + w * 4096, lb = la + 2048;
  *(LDS u32x4*)(lds + sreg + l * 16) = u32x4{0, 0, 0, 0};
  *(LDS u32x4*)(lds + sreg + 1024 + l * 16) = u32x4{0, 0, 0, 0};
  *(LDS u32x4*)(lds + lb + l * 16) = u32x4{0, 0, 0, 0};
  *(LDS u32x4*)(lds + lb + 1024 + l * 16) = u32x4{0, 0, 0, 0};
  asm volatile("s_waitcnt vmcnt(8)" ::: "memory");
  BAR();
  STAMP(1);

  int wb = 0, n, nw;
  unsigned cbase[8], mt[8];
  auto window_issue = [&]() {
    int l = tid & 63;
    asm volatile("" : "+v"(l));
    unsigned cn[4], rb[4], ix[4];
    unsigned tot = 0;
#pragma unroll
    for (int k = 0; k < 4; ++k) {
      const int c = l + 64 * k;
      const unsigned o0 = *(LDS unsigned short*)(lds + L_T1 + c * 16 + w * 2);
      const unsigned o1 = *(LDS unsigned short*)(lds + (w < 7 ? L_T1 + c * 16 + w * 2 + 2 : L_T2 + c * 4));
      cn[k] = c < NC ? o1 - o0 : 0u;
      rb[k] = c * CE + o0;
      ix[k] = tot;
      tot += cn[k];
    }
    const unsigned incl = wave_incl_scan(tot);
    n = __builtin_amdgcn_readlane(incl, 63);
    nw = n - wb < CAP ? n - wb : CAP;
    const int base = (int)(incl - tot) - wb;
#pragma unroll
    for (int k = 0; k < 4; ++k) {
      const int lo = base + (int)ix[k];
      const int pos = lo > 0 ? lo : 0;
      if (cn[k] != 0 && lo < CAP && lo + (int)cn[k] > 0) *(LDS unsigned*)(lds + lb + pos * 4) = ((pos + 1) << 20) | (rb[k] + 512 - lo);
    }
    asm volatile("" ::: "memory");
    const u32x4 m0 = *(LDS u32x4*)(lds + lb + l * 32), m1 = *(LDS u32x4*)(lds + lb + l * 32 + 16);
    unsigned run[8];
    run[0] = m0[0];
#pragma unroll
    for (int j = 1; j < 8; ++j) run[j] = umax(run[j - 1], j < 4 ? m0[j] : m1[j - 4]);
    const unsigned ex = wave_excl_max(run[7]);
#pragma unroll
    for (int j = 0; j < 8; ++j) {
      const int i = 8 * l + j;
      const unsigned rec = i < nw ? (umax(run[j], ex) & 0xFFFFFu) + (i - 512) : 0u;
      mt[j] = __builtin_amdgcn_raw_buffer_load_b32(sm, rec * 4, 0, 0);
      cbase[j] = __builtin_amdgcn_ubfe(__umul24(rec >> 7, 5243u), 17, 15);
    }
  };
  window_issue();
  STAMP(2);
  asm volatile("s_waitcnt vmcnt(8)" ::: "memory");
  BAR();

  bf16x8 W1[4][2], W3[2], P2[2][2], XO[4];
  {
    auto wfrag = [&](int f) { return *(LDS bf16x8*)(lds + L_STG + (f / 5) * 10240 + 5120 + (f % 5) * 1024 + l * 16); };
#pragma unroll
    for (int s = 0; s < 4; ++s) { W1[s][0] = wfrag(2 * s); W1[s][1] = wfrag(2 * s + 1); }
    W3[0] = wfrag(16); W3[1] = wfrag(17);
    const int rs = (r & 19) | ((r & 4) << 1) | ((r & 8) >> 1);
    f32x16 p0, p1;
#pragma unroll
    for (int i = 0; i < 16; ++i) { p0[i] = 0.f; p1[i] = 0.f; }
#pragma unroll
    for (int s = 0; s < 4; ++s) {
      const bf16x8 a = *(LDS bf16x8*)(lds + stg + rs * 128 + (((2 * s + h) ^ ((rs >> 1) & 7)) << 4));
      p0 = MFMA(a, wfrag(8 + 2 * s), p0);
      p1 = MFMA(a, wfrag(9 + 2 * s), p1);
      XO[s] = *(LDS bf16x8*)(lds + stg + r * 128 + (((2 * s + h) ^ ((r >> 1) & 7)) << 4));
    }
#pragma unroll
    for (int s = 0; s < 2; ++s) {
      P2[s][0] = __builtin_bit_cast(bf16x8, u32x4{pk(p0[8 * s], p0[8 * s + 1]), pk(p0[8 * s + 2], p0[8 * s + 3]), pk(p0[8 * s + 4], p0[8 * s + 5]), pk(p0[8 * s + 6], p0[8 * s + 7])});
      P2[s][1] = __builtin_bit_cast(bf16x8, u32x4{pk(p1[8 * s], p1[8 * s + 1]), pk(p1[8 * s + 2], p1[8 * s + 3]), pk(p1[8 * s + 4], p1[8 * s + 5]), pk(p1[8 * s + 6], p1[8 * s + 7])});
    }
  }
  f32x16 Y0, Y1;
#pragma unroll
  for (int i = 0; i < 16; ++i) { Y0[i] = 0.f; Y1[i] = 0.f; }

  unsigned xch[4];
#pragma unroll
  for (int s = 0; s < 4; ++s) xch[s] = ((l & 7) ^ (((8 * s + (l >> 3)) >> 1) & 7)) << 4;
  const unsigned ech = ((l & 1) ^ ((l >> 4) & 1)) << 4;
  const unsigned skey = sreg + (l >> 1) * 64 + (((l >> 3) & 3) << 4);
  const unsigned aoff = la + (l >> 3) * 16;
  const unsigned boff = lb + (4 * ((l >> 1) & 7) + (l >> 4)) * 4;
  unsigned xrd[4];
#pragma unroll
  for (int s = 0; s < 4; ++s) xrd[s] = stg + r * 128 + (((2 * s + h) ^ ((r >> 1) & 7)) << 4);
  const unsigned erd = stg + 4096 + r * 32 + ((h ^ ((r >> 3) & 1)) << 4);
  const unsigned srow0 = sreg + r * 64 + ((h ^ ((r >> 2) & 3)) << 4), srow1 = srow0 ^ 32;
  const unsigned str0 = sreg + (4 * h + ((l & 15) >> 2)) * 64 + (((2 * ((l >> 4) & 1) + ((l & 3) >> 1)) ^ h) << 4) + (l & 1) * 8;
  const unsigned str1 = (str0 ^ 32) + 512;

  BAR();
  STAMP(3);
  while (true) {
    const int nt = (nw + 31) >> 5;
    {
      int l = tid & 63;
      asm volatile("" : "+v"(l));
      unsigned av[8], bv[8];
#pragma unroll
      for (int j = 0; j < 8; ++j) {
        const int i = 8 * l + j;
        const unsigned sn = mt[j] / (unsigned)(GP * CE), rem = mt[j] - __umul24(sn, GP * CE);
        const unsigned dl = __umul24(rem >> 7, 5243u) >> 17, idx = rem - __umul24(dl, CE);
        av[j] = i < nw ? sn << 7 : 0u;
        bv[j] = i < nw ? ((__umul24(cbase[j], CE) + idx) << 5) | (dl << 27) : (31u << 27);
      }
      *(LDS u32x4*)(lds + la + l * 32) = u32x4{av[0], av[1], av[2], av[3]};
      *(LDS u32x4*)(lds + la + l * 32 + 16) = u32x4{av[4], av[5], av[6], av[7]};
      *(LDS u32x4*)(lds + lb + l * 32) = u32x4{bv[0], bv[1], bv[2], bv[3]};
      *(LDS u32x4*)(lds + lb + l * 32 + 16) = u32x4{bv[4], bv[5], bv[6], bv[7]};
      asm volatile("" ::: "memory");
    }
    STAMP(4); STAMPC(0, __builtin_amdgcn_s_memtime()); STAMPC(2, nt);
    auto dma_tile = [&](const int slot, const int tt, const u32x4 A, const unsigned B) {
      const unsigned v0 = A[0] | xch[0], v1 = A[1] | xch[1], v2 = A[2] | xch[2], v3 = A[3] | xch[3], v4 = (B & 0x07FFFFFFu) | ech;
      asm volatile("s_waitcnt lgkmcnt(0)\n\t"
                   "s_cmp_lt_i32 %[t], %[nt]\n\ts_cbranch_scc0 1f\n\t"
                   "s_mov_b32 m0, %[m]\n\ts_nop 0\n\tbuffer_load_dwordx4 %[v0], %[sx], 0 offen lds\n\t"
                   "s_add_u32 m0, %[m], 0x400\n\ts_nop 0\n\tbuffer_load_dwordx4 %[v1], %[sx], 0 offen lds\n\t"
                   "s_add_u32 m0, %[m], 0x800\n\ts_nop 0\n\tbuffer_load_dwordx4 %[v2], %[sx], 0 offen lds\n\t"
                   "s_add_u32 m0, %[m], 0xc00\n\ts_nop 0\n\tbuffer_load_dwordx4 %[v3], %[sx], 0 offen lds\n\t"
                   "s_add_u32 m0, %[m], 0x1000\n\ts_nop 0\n\tbuffer_load_dwordx4 %[v4], %[se], 0 offen " EPOL " lds\n\t"
                   "1:"
                   ::[t] "s"(tt), [nt] "s"(nt), [m] "s"(ldsb + stg + slot * 5120), [v0] "v"(v0), [v1] "v"(v1), [v2] "v"(v2), [v3] "v"(v3), [v4] "v"(v4), [sx] "s"(sx), [se] "s"(se)
                   : "m0", "scc", "memory");
    };
    auto wait_dma = [&](const int tt) {
      asm volatile("s_cmp_lt_i32 %0, %1\n\ts_cbranch_scc1 1f\n\ts_waitcnt vmcnt(0)\n\t1:\n\ts_waitcnt vmcnt(5)" ::"s"(tt), "s"(nt) : "scc", "memory");
    };
    f32x16 a0, a1;
    bf16x8 xf[4], ef, sr0, sr1;
    s16x4 q00 = {0, 0, 0, 0}, q01 = q00, q10 = q00, q11 = q00;
    u32x4 mm0 = {0, 0, 0, 0}, mm1 = mm0, A4;
    unsigned Bq[2], sa;
    f32x16 zero;
#pragma unroll
    for (int i = 0; i < 16; ++i) zero[i] = 0.f;
    auto rd_frags = [&](const int slot) {
#pragma unroll
      for (int s = 0; s < 4; ++s) xf[s] = *(LDS bf16x8*)(lds + xrd[s] + slot * 5120);
      ef = *(LDS bf16x8*)(lds + erd + slot * 5120);
      sr0 = *(LDS bf16x8*)(lds + srow0); sr1 = *(LDS bf16x8*)(lds + srow1);
    };
    asm volatile("" ::: "memory");
    {
      A4 = *(LDS u32x4*)(lds + aoff); Bq[0] = *(LDS unsigned*)(lds + boff);
      const u32x4 A1 = *(LDS u32x4*)(lds + aoff + 128); Bq[1] = *(LDS unsigned*)(lds + boff + 128);
      dma_tile(0, 0, A4, Bq[0]);
      dma_tile(1, 1, A1, Bq[1]);
      wait_dma(1);
      sa = (Bq[0] >> 26) ^ skey;
      *(LDS unsigned short*)(lds + sa) = 0x3F80;
      asm volatile("" ::: "memory");
      rd_frags(0);
      A4 = *(LDS u32x4*)(lds + aoff + 256); Bq[0] = *(LDS unsigned*)(lds + boff + 256);
      dma_tile(0, 2, A4, Bq[0]);
      a0 = MFMA(sr0, P2[0][0], zero); a0 = MFMA(sr1, P2[1][0], a0);
#pragma unroll
      for (int s = 0; s < 4; ++s) a0 = MFMA(xf[s], W1[s][0], a0);
      a0 = MFMA(ef, W3[0], a0);
    }
#define S0_ __builtin_bit_cast(bf16x8, __builtin_shufflevector(q00, q01, 0, 1, 2, 3, 4, 5, 6, 7))
#define S1_ __builtin_bit_cast(bf16x8, __builtin_shufflevector(q10, q11, 0, 1, 2, 3, 4, 5, 6, 7))
#define BF(v) __builtin_bit_cast(bf16x8, v)
#define PIN(lo, hi) _Pragma("unroll") for (int i = lo; i < hi; i += 4) asm volatile("" : "+v"(u[i]), "+v"(u[i + 1]), "+v"(u[i + 2]), "+v"(u[i + 3])); SB();
#define MULS(lo, hi) _Pragma("unroll") for (int i = lo; i < hi; ++i) u[i] = c[i] * -1.4426950408889634f; PIN(lo, hi)
#define EXPS(lo, hi) _Pragma("unroll") for (int i = lo; i < hi; ++i) u[i] = __builtin_amdgcn_exp2f(u[i]); PIN(lo, hi)
#define ADDS(lo, hi) _Pragma("unroll") for (int i = lo; i < hi; ++i) u[i] = 1.0f + u[i]; PIN(lo, hi)
#define RCPS(lo, hi) _Pragma("unroll") for (int i = lo; i < hi; ++i) u[i] = __builtin_amdgcn_rcpf(u[i]); PIN(lo, hi)
#define FINS(lo, hi) _Pragma("unroll") for (int i = lo; i < hi; ++i) u[i] *= c[i]; PIN(lo, hi)
    auto phase = [&](const int T, const int par, const int t, f32x16& c, f32x16& nx, f32x16& Yc, f32x16& Yp) {
      float u[16];
      SB();
      MULS(0, 8)
      Yp = MFMA(BF(mm1), S1_, Yp); SB();
      MULS(8, 16)
      if (T == 0) {
        q00 = __builtin_amdgcn_ds_read_tr16_b64_v4i16((LDS s16x4*)(lds + str0));
        q01 = __builtin_amdgcn_ds_read_tr16_b64_v4i16((LDS s16x4*)(lds + str1));
        q10 = __builtin_amdgcn_ds_read_tr16_b64_v4i16((LDS s16x4*)(lds + str0 + 1024));
        q11 = __builtin_amdgcn_ds_read_tr16_b64_v4i16((LDS s16x4*)(lds + str1 + 1024));
        asm volatile("" ::: "memory");
        *(LDS unsigned short*)(lds + sa) = 0;
        sa = (Bq[par ^ 1] >> 26) ^ skey;
        SB();
      } else {
        dma_tile(par ^ 1, t + 3, A4, Bq[par ^ 1]);
        SB();
      }
      const int U = T ^ 1;
      nx = MFMA(sr0, P2[0][U], zero); SB();
      EXPS(0, 4)
      nx = MFMA(sr1, P2[1][U], nx); SB();
      EXPS(4, 8)
      nx = MFMA(xf[0], W1[0][U], nx); SB();
      EXPS(8, 12)
      nx = MFMA(xf[1], W1[1][U], nx); SB();
      EXPS(12, 16)
      nx = MFMA(xf[2], W1[2][U], nx); SB();
      ADDS(0, 16)
      nx = MFMA(xf[3], W1[3][U], nx); SB();
      RCPS(0, 4)
      nx = MFMA(ef, W3[U], nx); SB();
      if (T == 0) {
        wait_dma(t + 2);
        *(LDS unsigned short*)(lds + sa) = 0x3F80;
        asm volatile("" ::: "memory");
        rd_frags(par ^ 1);
        A4 = *(LDS u32x4*)(lds + aoff + (t + 3) * 128);
        Bq[par ^ 1] = *(LDS unsigned*)(lds + boff + (t + 3) * 128);
        SB();
      }
      RCPS(4, 16)
      FINS(0, 16)
      mm0 = u32x4{pk(u[0], u[1]), pk(u[2], u[3]), pk(u[4], u[5]), pk(u[6], u[7])}; SB();
      mm1 = u32x4{pk(u[8], u[9]), pk(u[10], u[11]), pk(u[12], u[13]), pk(u[14], u[15])}; SB();
      Yc = MFMA(BF(mm0), S0_, Yc); SB();
    };
    for (int t = 0; t < nt; t += 2) {
      phase(0, 0, t, a0, a1, Y0, Y1);
      phase(1, 0, t, a1, a0, Y1, Y0);
      if (t + 1 >= nt) break;
      phase(0, 1, t + 1, a0, a1, Y0, Y1);
      phase(1, 1, t + 1, a1, a0, Y1, Y0);
    }
    Y1 = MFMA(BF(mm1), S1_, Y1);
    *(LDS unsigned short*)(lds + sa) = 0;
    wb += CAP;
    if (wb >= n) break;
    *(LDS u32x4*)(lds + lb + l * 16) = u32x4{0, 0, 0, 0};
    *(LDS u32x4*)(lds + lb + 1024 + l * 16) = u32x4{0, 0, 0, 0};
    asm volatile("" ::: "memory");
    window_issue();
  }

  STAMP(5); STAMPC(1, __builtin_amdgcn_s_memtime());
  f32x16 o0, o1;
#pragma unroll
  for (int i = 0; i < 16; ++i) { o0[i] = 0.f; o1[i] = 0.f; }
  auto ufrag = [&](int f) { return *(LDS bf16x8*)(lds + L_WU + f * 1024 + l * 16); };
#pragma unroll
  for (int s = 0; s < 4; ++s) { o0 = MFMA(ufrag(2 * s), XO[s], o0); o1 = MFMA(ufrag(2 * s + 1), XO[s], o1); }
#pragma unroll
  for (int s = 0; s < 2; ++s) {
    const bf16x8 z0 = __builtin_bit_cast(bf16x8, u32x4{pk(Y0[8 * s], Y0[8 * s + 1]), pk(Y0[8 * s + 2], Y0[8 * s + 3]), pk(Y0[8 * s + 4], Y0[8 * s + 5]), pk(Y0[8 * s + 6], Y0[8 * s + 7])});
    const bf16x8 z1 = __builtin_bit_cast(bf16x8, u32x4{pk(Y1[8 * s], Y1[8 * s + 1]), pk(Y1[8 * s + 2], Y1[8 * s + 3]), pk(Y1[8 * s + 4], Y1[8 * s + 5]), pk(Y1[8 * s + 6], Y1[8 * s + 7])});
    o0 = MFMA(ufrag(8 + 2 * s), z0, o0); o1 = MFMA(ufrag(9 + 2 * s), z0, o1);
    o0 = MFMA(ufrag(12 + 2 * s), z1, o0); o1 = MFMA(ufrag(13 + 2 * s), z1, o1);
  }
#pragma unroll
  for (int q = 0; q < 4; ++q) {
    const u32x2 v0 = {pk(silu1(o0[4 * q]), silu1(o0[4 * q + 1])), pk(silu1(o0[4 * q + 2]), silu1(o0[4 * q + 3]))};
    const u32x2 v1 = {pk(silu1(o1[4 * q]), silu1(o1[4 * q + 1])), pk(silu1(o1[4 * q + 2]), silu1(o1[4 * q + 3]))};
    *(LDS u32x2*)(lds + stg + r * 128 + ((q ^ (r & 7)) << 4) + h * 8) = v0;
    *(LDS u32x2*)(lds + stg + r * 128 + (((4 + q) ^ (r & 7)) << 4) + h * 8) = v1;
  }
  asm volatile("" ::: "memory");
#pragma unroll
  for (int it = 0; it < 4; ++it) {
    const int m = l + 64 * it, row = m >> 3, p = m & 7;
    if (m < GP * 8) {
      const u32x4 v = *(LDS u32x4*)(lds + stg + row * 128 + ((p ^ (row & 7)) << 4));
      *(u32x4*)(out + (size_t)(GP * g) * 128 + m * 16) = v;
    }
  }
  STAMP(6);
}

static inline void launch_part(void* const* d_in, void* d_ws, hipStream_t stream) {
  k_part<<<NC + 3, 1024, 0, stream>>>((const i32x4*)d_in[3], (const i32x4*)d_in[2], (const u32x4*)d_in[1], (const uint16_t*)d_in[4], (const uint16_t*)d_in[5], (char*)d_ws);
}
static inline void launch_main(void* const* d_in, void* d_out, void* d_ws, hipStream_t stream) {
  k_main<<<NB, 512, 0, stream>>>((const char*)d_in[0], (const char*)d_in[1], (const char*)d_ws, (char*)d_out);
}

extern "C" void kernel_launch(void* const* d_in, const int* in_sizes, int n_in, void* d_out, int out_size, void* d_ws, size_t ws_size,
                              hipStream_t stream) {
  launch_part(d_in, d_ws, stream);
  launch_main(d_in, d_out, d_ws, stream);
}
```

```cpp
#include <hip/hip_runtime.h>
#include <stdint.h>

#define LDS __attribute__((address_space(3)))
#define DI __device__ __forceinline__
typedef float f32x16 __attribute__((ext_vector_type(16)));
typedef float f32x2 __attribute__((ext_vector_type(2)));
typedef unsigned u32x4 __attribute__((ext_vector_type(4)));
typedef unsigned u32x2 __attribute__((ext_vector_type(2)));
typedef int i32x4 __attribute__((ext_vector_type(4)));
typedef __bf16 bf16x8 __attribute__((ext_vector_type(8)));
typedef __bf16 bf16x2 __attribute__((ext_vector_type(2)));
typedef short s16x4 __attribute__((ext_vector_type(4)));

constexpr int NN = 50000;
constexpr int NE = 800000;
constexpr int GP = 25;
constexpr int CE = 3200;
constexpr int NC = 250;
constexpr int NB = 250;
constexpr int CAP = 512;
constexpr unsigned WS_EP = 0, WS_META = 25600000u, WS_OFF = 28800000u, WS_WF = 29824000u;

#define MFMA(a, b, c) __builtin_amdgcn_mfma_f32_32x32x16_bf16((a), (b), (c), 0, 0, 0)
#define BAR() asm volatile("s_waitcnt lgkmcnt(0)\n\ts_barrier" ::: "memory")
#define SB() __builtin_amdgcn_sched_barrier(0)
#define EPOL "nt"
#define STAMP(k)
#define STAMPC(k, v)

DI unsigned pk(float a, float b) { f32x2 v = {a, b}; bf16x2 r = __builtin_convertvector(v, bf16x2); return __builtin_bit_cast(unsigned, r); }
DI unsigned wave_incl_scan(unsigned x) {
  x += __builtin_amdgcn_update_dpp(0u, x, 0x111, 0xf, 0xf, false);
  x += __builtin_amdgcn_update_dpp(0u, x, 0x112, 0xf, 0xf, false);
  x += __builtin_amdgcn_update_dpp(0u, x, 0x114, 0xf, 0xf, false);
  x += __builtin_amdgcn_update_dpp(0u, x, 0x118, 0xf, 0xf, false);
  x += __builtin_amdgcn_update_dpp(0u, x, 0x142, 0xa, 0xf, false);
  x += __builtin_amdgcn_update_dpp(0u, x, 0x143, 0xc, 0xf, false);
  return x;
}

__global__ __launch_bounds__(512) void k_part(const i32x4* __restrict__ dst4, const i32x4* __restrict__ src4, const u32x4* __restrict__ x16,
                                              const uint16_t* __restrict__ wm, const uint16_t* __restrict__ wu, char* __restrict__ ws) {
  __shared__ __attribute__((aligned(16))) unsigned cnt[2048];
  __shared__ __attribute__((aligned(16))) unsigned metaL[CE];
  __shared__ unsigned wsum[8];
  const int tid = threadIdx.x, c = blockIdx.x;
#undef PROFBUF
#define PROFBUF (ws + (8u << 20))
  if (c >= NC) {
    const int it = (c - NC) * 512 + tid;
    if (it < 34 * 64) {
      const int f = it >> 6, l = it & 63, col = l & 31, h = l >> 5;
      unsigned short v[8];
#pragma unroll
      for (int j = 0; j < 8; ++j) {
        if (f < 18) {
          const int t = f & 1, s = (f >> 1) & 3;
          const int row = (f < 8 ? 0 : (f < 16 ? 64 : 128)) + (f < 16 ? 16 * s : 0) + 8 * h + j;
          v[j] = wm[row * 64 + 32 * t + col];
        } else {
          const int ks = (f - 18) >> 1, u = f & 1;
          const int row = ks < 4 ? 16 * ks + 8 * h + j : 64 + 32 * ((ks - 4) >> 1) + 16 * (ks & 1) + 8 * (j >> 2) + 4 * h + (j & 3);
          v[j] = wu[row * 64 + 32 * u + col];
        }
      }
      u32x4 o = {v[0] | ((unsigned)v[1] << 16), v[2] | ((unsigned)v[3] << 16), v[4] | ((unsigned)v[5] << 16), v[6] | ((unsigned)v[7] << 16)};
      *(u32x4*)(ws + WS_WF + it * 16) = o;
    }
    return;
  }
  STAMP(0);
  constexpr int TA = CE / 8;
  i32x4 d[2], s[2];
  const int ta = tid < TA ? tid : TA - 1;
#pragma unroll
  for (int i = 0; i < 2; ++i) { d[i] = dst4[c * (CE / 4) + ta + i * TA]; s[i] = src4[c * (CE / 4) + ta + i * TA]; }
  *(u32x4*)&cnt[4 * tid] = u32x4{0, 0, 0, 0};
  BAR();
  STAMP(1);
  unsigned g[8], rk[8];
  if (tid < TA) {
#pragma unroll
    for (int j = 0; j < 8; ++j) { g[j] = (unsigned)d[j >> 2][j & 3] / (unsigned)GP; rk[j] = __hip_atomic_fetch_add(&cnt[g[j]], 1u, __ATOMIC_RELAXED, __HIP_MEMORY_SCOPE_WORKGROUP); }
  }
  BAR();
  STAMP(2);
  const u32x4 cc = *(const u32x4*)&cnt[4 * tid];
  const unsigned tot = cc[0] + cc[1] + cc[2] + cc[3];
  const unsigned incl = wave_incl_scan(tot);
  if ((tid & 63) == 63) wsum[tid >> 6] = incl;
  BAR();
  unsigned ws8 = wsum[tid & 7];
  ws8 += __builtin_amdgcn_update_dpp(0u, ws8, 0x111, 0xf, 0xf, false);
  ws8 += __builtin_amdgcn_update_dpp(0u, ws8, 0x112, 0xf, 0xf, false);
  ws8 += __builtin_amdgcn_update_dpp(0u, ws8, 0x114, 0xf, 0xf, false);
  const int w = __builtin_amdgcn_readfirstlane(tid >> 6);
  const unsigned wb = w ? __builtin_amdgcn_readlane(ws8, w - 1) : 0u;
  const unsigned o0 = wb + incl - tot, o1 = o0 + cc[0], o2 = o1 + cc[1], o3 = o2 + cc[2];
  *(u32x4*)&cnt[4 * tid] = u32x4{o0, o1, o2, o3};
  *(u32x2*)(ws + WS_OFF + c * 4096 + tid * 8) = u32x2{o0 | (o1 << 16), o2 | (o3 << 16)};
  BAR();
  STAMP(3);
  if (tid < TA) {
#pragma unroll
    for (int j = 0; j < 8; ++j) {
      const unsigned dv = d[j >> 2][j & 3], sv = s[j >> 2][j & 3];
      metaL[cnt[g[j]] + rk[j]] = sv * (GP * CE) + (dv - g[j] * GP) * CE + (4 * tid + (j & 3) + (j >> 2) * (4 * TA));
    }
  }
  BAR();
  STAMP(4);
  if (tid < TA) {
#pragma unroll
    for (int i = 0; i < 2; ++i) *(u32x4*)(ws + WS_META + c * (CE * 4) + (tid + i * TA) * 16) = *(const u32x4*)&metaL[4 * (tid + i * TA)];
  }
  STAMP(5);
  STAMP(6);
}

constexpr unsigned L_STG = 0;
constexpr unsigned SLOT = 40960;
constexpr unsigned L_S = 81920;
constexpr unsigned L_LIST = 98304;
constexpr unsigned L_WU = 131072;
constexpr unsigned L_T1 = 147456;
constexpr unsigned L_T2 = 151552;
constexpr unsigned L_PRG = 155648;
constexpr unsigned L_END = 158720;
constexpr unsigned L_M = 0;
constexpr unsigned L_OWN = 40960;
constexpr unsigned L_WIMG = 73728;

DI void dma16(unsigned m0, unsigned voff, __amdgpu_buffer_rsrc_t srd, unsigned soff) {
  asm volatile("s_mov_b32 m0, %0\n\ts_nop 0\n\tbuffer_load_dwordx4 %1, %2, %3 offen lds" ::"s"(m0), "v"(voff), "s"(srd), "s"(soff) : "m0", "memory");
}
DI unsigned umax(unsigned a, unsigned b) { return a > b ? a : b; }
DI unsigned wave_excl_max(unsigned x) {
  x = umax(x, __builtin_amdgcn_update_dpp(0u, x, 0x111, 0xf, 0xf, false));
  x = umax(x, __builtin_amdgcn_update_dpp(0u, x, 0x112, 0xf, 0xf, false));
  x = umax(x, __builtin_amdgcn_update_dpp(0u, x, 0x114, 0xf, 0xf, false));
  x = umax(x, __builtin_amdgcn_update_dpp(0u, x, 0x118, 0xf, 0xf, false));
  x = umax(x, __builtin_amdgcn_update_dpp(0u, x, 0x142, 0xa, 0xf, false));
  x = umax(x, __builtin_amdgcn_update_dpp(0u, x, 0x143, 0xc, 0xf, false));
  return __builtin_amdgcn_update_dpp(0u, x, 0x138, 0xf, 0xf, false);
}
DI float silu1(float v) { return v * __builtin_amdgcn_rcpf(1.0f + __builtin_amdgcn_exp2f(v * -1.4426950408889634f)); }

__global__ __launch_bounds__(512, 2) void k_main(const char* __restrict__ x, const char* __restrict__ ea, const char* __restrict__ ws, char* __restrict__ out) {
  __shared__ __attribute__((aligned(1024))) char lds_[L_END];
  LDS char* lds = (LDS char*)lds_;
  const unsigned ldsb = (unsigned)(size_t)lds;
  const int tid = threadIdx.x, l = tid & 63, w = __builtin_amdgcn_readfirstlane(tid >> 6);
  const int xcd = blockIdx.x & 7, b = xcd * 31 + (xcd < 2 ? xcd : 2) + (blockIdx.x >> 3);
  const int g = 8 * b + w;
  const int r = l & 31, h = l >> 5;
#undef PROFBUF
#define PROFBUF ((char*)ws)
  STAMP(0);
  const auto sx = __builtin_amdgcn_make_buffer_rsrc((void*)x, 0, NN * 128, 0x00020000);
  const auto se = __builtin_amdgcn_make_buffer_rsrc((void*)ea, 0, NE * 32, 0x00020000);
  const auto sm = __builtin_amdgcn_make_buffer_rsrc((void*)(ws + WS_META), 0, NE * 4, 0x00020000);
  const auto sw = __builtin_amdgcn_make_buffer_rsrc((void*)(ws + WS_WF), 0, 34 * 1024, 0x00020000);

  const auto st = __builtin_amdgcn_make_buffer_rsrc((void*)(ws + WS_OFF), 0, NC * 4096, 0x00020000);

  const unsigned stg = L_STG + w * 5120;
  asm volatile("s_nop 4" ::: "memory");
  dma16(ldsb + L_T1 + w * 1024, (tid & 255) * 4096 + b * 16 + (w >> 2) * 16, st, 0);
  const unsigned sreg = L_S + w * 2048;
  const unsigned la = L_LIST + w * 4096, lb = la + 2048;
  *(LDS u32x4*)(lds + lb + l * 16) = u32x4{512, 512, 512, 512};
  *(LDS u32x4*)(lds + lb + 1024 + l * 16) = u32x4{512, 512, 512, 512};
  f32x16 Y0, Y1;
#pragma unroll
  for (int i = 0; i < 16; ++i) { Y0[i] = 0.f; Y1[i] = 0.f; }

  unsigned xch[4];
#pragma unroll
  for (int s = 0; s < 4; ++s) xch[s] = ((l & 7) ^ (((8 * s + (l >> 3)) >> 1) & 7)) << 4;
  unsigned ech = ((l & 1) ^ ((l >> 4) & 1)) << 4;
  unsigned skey = sreg + (l >> 1) * 64 + (((l >> 3) & 3) << 4);
  unsigned aoff = la + (l >> 3) * 16;
  unsigned boff = lb + (4 * ((l >> 1) & 7) + (l >> 4)) * 4;
  unsigned xrd[4];
#pragma unroll
  for (int s = 0; s < 4; ++s) xrd[s] = stg + r * 128 + (((2 * s + h) ^ ((r >> 1) & 7)) << 4);
  unsigned erd = stg + 4096 + r * 32 + ((h ^ ((r >> 3) & 1)) << 4);
  unsigned srow0 = sreg + r * 64 + ((h ^ ((r >> 2) & 3)) << 4), srow1 = srow0 ^ 32;
  unsigned str0 = sreg + (4 * h + ((l & 15) >> 2)) * 64 + (((2 * ((l >> 4) & 1) + ((l & 3) >> 1)) ^ h) << 4) + (l & 1) * 8;
  unsigned str1 = (str0 ^ 32) + 512;

  asm volatile("" : "+v"(xch[0]), "+v"(xch[1]), "+v"(ech), "+v"(skey), "+v"(aoff), "+v"(boff), "+v"(xrd[0]), "+v"(xrd[1]), "+v"(xrd[2]), "+v"(xrd[3]));
  asm volatile("" : "+v"(erd), "+v"(srow0), "+v"(srow1), "+v"(str0), "+v"(str1), "+v"(Y0), "+v"(Y1));
  xch[2] = xch[0]; xch[3] = xch[1];
  asm volatile("s_waitcnt vmcnt(0)" ::: "memory");
  BAR();
  STAMP(1);
  {
    const unsigned c0 = 32 * w + (l >> 3);
    unsigned s0[4], s1[4], vo[4];
#pragma unroll
    for (int i = 0; i < 4; ++i) { s0[i] = *(LDS unsigned short*)(lds + L_T1 + c0 * 16 + i * 128); s1[i] = *(LDS unsigned short*)(lds + L_T2 + c0 * 16 + i * 128); }
#pragma unroll
    for (int i = 0; i < 4; ++i) {
      const unsigned o = (s0[i] & ~3u) + 4 * (l & 7);
      vo[i] = o < s1[i] ? (__umul24(c0 + 8 * i, CE) + o) * 4 : 0x7FFFFFF0u;
    }
#pragma unroll
    for (int i = 0; i < 4; ++i) dma16(ldsb + L_M + (32 * w + 8 * i) * 128, vo[i], sm, 0);
  }

  int wb = 0, n, nw;
  unsigned cj[8], mt[8];
  auto window_scan = [&]() {
    int l = tid & 63;
    asm volatile("" : "+v"(l));
    unsigned s0[4], o0[4], o1[4], cn[4], rb[4], ix[4];
    const unsigned ta = L_T1 + l * 16, tb = ta + w * 2, tc = l * 16 + (w < 7 ? L_T1 + w * 2 + 2 : L_T2);
#pragma unroll
    for (int k = 0; k < 4; ++k) {
      s0[k] = *(LDS unsigned short*)(lds + ta + k * 1024);
      o0[k] = *(LDS unsigned short*)(lds + tb + k * 1024);
      o1[k] = *(LDS unsigned short*)(lds + tc + k * 1024);
    }
    unsigned tot = 0;
#pragma unroll
    for (int k = 0; k < 4; ++k) {
      cn[k] = o1[k] - o0[k];
      if (k == 3) cn[k] = l < NC - 192 ? cn[k] : 0u;
      rb[k] = (l << 13) + o0[k] - (s0[k] & ~3u) + ((k << 19) + 512 + (1 << 21));
      ix[k] = tot;
      tot += cn[k];
    }
    const unsigned incl = wave_incl_scan(tot);
    n = __builtin_amdgcn_readlane(incl, 63);
    nw = n - wb < CAP ? n - wb : CAP;
    const int base = (int)(incl - tot) - wb;
#pragma unroll
    for (int k = 0; k < 4; ++k) {
      const int lo = base + (int)ix[k];
      const int pos = lo > 0 ? lo : 0;
      const bool on = pos < lo + (int)cn[k] && pos < CAP;
      *(LDS unsigned*)(lds + (on ? lb + pos * 4 : la + l * 4)) = (pos << 21) + (rb[k] - lo);
    }
    asm volatile("" ::: "memory");
    const u32x4 m0 = *(LDS u32x4*)(lds + lb + l * 32), m1 = *(LDS u32x4*)(lds + lb + l * 32 + 16);
    unsigned run[8];
    run[0] = m0[0];
#pragma unroll
    for (int j = 1; j < 8; ++j) run[j] = umax(run[j - 1], j < 4 ? m0[j] : m1[j - 4]);
    const unsigned ex = wave_excl_max(run[7]);
    const int last = (nw > 0 ? nw - 1 : 0) - 512, i0 = 8 * l - 512;
#pragma unroll
    for (int j = 0; j < 8; ++j) {
      const int im = i0 < last - j ? i0 : last - j;
      cj[j] = (umax(run[j], ex) & 0x1FFFFFu) + im + j;
    }
  };
  auto window_gather = [&](bool all) {
#pragma unroll
    for (int j = 0; j < 8; ++j) {
      const unsigned c = cj[j] >> 13, q = cj[j] & 0x1FFFu;
      const unsigned s0 = *(LDS unsigned short*)(lds + L_T1 + c * 16) & ~3u;
      const unsigned v = __builtin_amdgcn_raw_buffer_load_b32(sm, (__umul24(c, CE) + s0 + q) * 4, 0, 0);
      mt[j] = (all || q >= 32) ? v : mt[j];
    }
  };
  window_scan();
  unsigned pa = L_PRG + w * 256 + l * 4;
  asm volatile("" : "+v"(pa));
  *(LDS int*)(lds + pa) = n; *(LDS int*)(lds + pa + 2048) = n;
#pragma unroll
  for (int i = 0; i < 5; ++i) {
    const int f = w + 8 * i;
    const unsigned dstl = f < 18 ? L_WIMG + f * 1024 : L_WU + (f - 18) * 1024;
    if (f < 34) dma16(ldsb + dstl, l * 16, sw, f * 1024);
  }
  const unsigned own = L_OWN + w * 4096;
#pragma unroll
  for (int s = 0; s < 4; ++s) {
    const int R = 8 * s + (l >> 3);
    int node = GP * g + R; node = node < NN ? node : NN - 1;
    dma16(ldsb + own + s * 1024, node * 128 + (((l & 7) ^ ((R >> 1) & 7)) << 4), sx, 0);
  }

  STAMP(2);
  asm volatile("s_waitcnt vmcnt(8)" ::: "memory");
  BAR();
  {
    unsigned acc = 0;
#pragma unroll
    for (int j = 0; j < 8; ++j) {
      acc |= cj[j];
      mt[j] = *(LDS unsigned*)(lds + L_M + (((cj[j] >> 13) << 5) + (cj[j] & 31)) * 4);
    }
    const bool over = (acc & 0x1FE0u) != 0;
    BAR();
    if (__any(over)) window_gather(false);
  }
  int nt;
  u32x4 A4;
  unsigned Bq[2];
  auto dma_tile = [&](const int slot, const int tt, const u32x4 A, const unsigned B) {
    const unsigned v0 = A[0] | xch[0], v1 = A[1] | xch[1], v2 = A[2] | xch[2], v3 = A[3] | xch[3], v4 = (B & 0x07FFFFFFu) | ech;
    asm volatile("s_waitcnt lgkmcnt(0)\n\t"
                 "s_cmp_lt_i32 %[t], %[nt]\n\ts_cbranch_scc0 1f\n\t"
                 "s_mov_b32 m0, %[m]\n\ts_nop 0\n\tbuffer_load_dwordx4 %[v0], %[sx], 0 offen lds\n\t"
                 "s_add_u32 m0, %[m], 0x400\n\ts_nop 0\n\tbuffer_load_dwordx4 %[v1], %[sx], 0 offen lds\n\t"
                 "s_add_u32 m0, %[m], 0x800\n\ts_nop 0\n\tbuffer_load_dwordx4 %[v2], %[sx], 0 offen lds\n\t"
                 "s_add_u32 m0, %[m], 0xc00\n\ts_nop 0\n\tbuffer_load_dwordx4 %[v3], %[sx], 0 offen lds\n\t"
                 "s_add_u32 m0, %[m], 0x1000\n\ts_nop 0\n\tbuffer_load_dwordx4 %[v4], %[se], 0 offen " EPOL " lds\n\t"
                 "1:"
                 ::[t] "s"(tt), [nt] "s"(nt), [m] "s"(ldsb + stg + slot * SLOT), [v0] "v"(v0), [v1] "v"(v1), [v2] "v"(v2), [v3] "v"(v3), [v4] "v"(v4), [sx] "s"(sx), [se] "s"(se)
                 : "m0", "scc", "memory");
  };
  auto wait_dma = [&](const int tt) {
    asm volatile("s_cmp_lt_i32 %0, %1\n\ts_cbranch_scc1 1f\n\ts_waitcnt vmcnt(0)\n\t1:\n\ts_waitcnt vmcnt(5)" ::"s"(tt), "s"(nt) : "scc", "memory");
  };
  auto window_lists = [&]() {
    nt = (nw + 31) >> 5;
    {
      int l = tid & 63;
      asm volatile("" : "+v"(l));
      unsigned av[8], bv[8];
#pragma unroll
      for (int j = 0; j < 8; ++j) {
        const unsigned t = mt[j] >> 7, sn = __umulhi(t, 54975582u) >> 3;
        const unsigned u = (unsigned)__mul24((int)sn, -625) + t;
        const unsigned dl = __umul24(u, 41u) >> 10;
        const unsigned eh = (unsigned)__mul24((int)dl, -25) + __umul24(cj[j] >> 13, 25u) + u;
        av[j] = sn << 7;
        bv[j] = (eh << 12) | ((mt[j] << 5) & 0xFE0u) | ((8 * l + j < nw ? dl : 31u) << 27);
      }
      *(LDS u32x4*)(lds + la + l * 32) = u32x4{av[0], av[1], av[2], av[3]};
      *(LDS u32x4*)(lds + la + l * 32 + 16) = u32x4{av[4], av[5], av[6], av[7]};
      *(LDS u32x4*)(lds + lb + l * 32) = u32x4{bv[0], bv[1], bv[2], bv[3]};
      *(LDS u32x4*)(lds + lb + l * 32 + 16) = u32x4{bv[4], bv[5], bv[6], bv[7]};
      asm volatile("" ::: "memory");
    }
    A4 = *(LDS u32x4*)(lds + aoff); Bq[0] = *(LDS unsigned*)(lds + boff);
    dma_tile(0, 0, A4, Bq[0]);
  };
  window_lists();
  wait_dma(0);
  BAR();
  STAMP(3);

  bf16x8 W1[4][2], W3[2], P2[2][2], XO[4];
  {
    auto wfrag = [&](int f) { return *(LDS bf16x8*)(lds + L_WIMG + f * 1024 + l * 16); };
#pragma unroll
    for (int s = 0; s < 4; ++s) { W1[s][0] = wfrag(2 * s); W1[s][1] = wfrag(2 * s + 1); }
    W3[0] = wfrag(16); W3[1] = wfrag(17);
    const int rs = (r & 19) | ((r & 4) << 1) | ((r & 8) >> 1);
    f32x16 p0, p1;
#pragma unroll
    for (int i = 0; i < 16; ++i) { p0[i] = 0.f; p1[i] = 0.f; }
#pragma unroll
    for (int s = 0; s < 4; ++s) {
      const bf16x8 a = *(LDS bf16x8*)(lds + own + rs * 128 + (((2 * s + h) ^ ((rs >> 1) & 7)) << 4));
      p0 = MFMA(a, wfrag(8 + 2 * s), p0);
      p1 = MFMA(a, wfrag(9 + 2 * s), p1);
      XO[s] = *(LDS bf16x8*)(lds + own + r * 128 + (((2 * s + h) ^ ((r >> 1) & 7)) << 4));
    }
#pragma unroll
    for (int s = 0; s < 2; ++s) {
      P2[s][0] = __builtin_bit_cast(bf16x8, u32x4{pk(p0[8 * s], p0[8 * s + 1]), pk(p0[8 * s + 2], p0[8 * s + 3]), pk(p0[8 * s + 4], p0[8 * s + 5]), pk(p0[8 * s + 6], p0[8 * s + 7])});
      P2[s][1] = __builtin_bit_cast(bf16x8, u32x4{pk(p1[8 * s], p1[8 * s + 1]), pk(p1[8 * s + 2], p1[8 * s + 3]), pk(p1[8 * s + 4], p1[8 * s + 5]), pk(p1[8 * s + 6], p1[8 * s + 7])});
    }
  }
  BAR();
  *(LDS u32x4*)(lds + sreg + l * 16) = u32x4{0, 0, 0, 0};
  *(LDS u32x4*)(lds + sreg + 1024 + l * 16) = u32x4{0, 0, 0, 0};
  while (true) {
    STAMP(4); STAMPC(0, __builtin_amdgcn_s_memtime()); STAMPC(2, nt);
    f32x16 a0, a1;
    bf16x8 xf[4], ef, sr0, sr1;
    s16x4 q00 = {0, 0, 0, 0}, q01 = q00, q10 = q00, q11 = q00;
    u32x4 mm0 = {0, 0, 0, 0}, mm1 = mm0;
    unsigned sa;
    int oth, mine;
    f32x16 zero;
#pragma unroll
    for (int i = 0; i < 16; ++i) zero[i] = 0.f;
    auto rd_frags = [&](const int slot) {
#pragma unroll
      for (int s = 0; s < 4; ++s) xf[s] = *(LDS bf16x8*)(lds + xrd[s] + slot * SLOT);
      ef = *(LDS bf16x8*)(lds + erd + slot * SLOT);
      sr0 = *(LDS bf16x8*)(lds + srow0); sr1 = *(LDS bf16x8*)(lds + srow1);
    };
    asm volatile("" ::: "memory");
    {
      const u32x4 A1 = *(LDS u32x4*)(lds + aoff + 128); Bq[1] = *(LDS unsigned*)(lds + boff + 128);
      dma_tile(1, 1, A1, Bq[1]);
      wait_dma(1);
      sa = (Bq[0] >> 26) ^ skey;
      *(LDS unsigned short*)(lds + sa) = 0x3F80;
      asm volatile("" ::: "memory");
      rd_frags(0);
      A4 = *(LDS u32x4*)(lds + aoff + 256); Bq[0] = *(LDS unsigned*)(lds + boff + 256);
      dma_tile(0, 2, A4, Bq[0]);
      a0 = MFMA(sr0, P2[0][0], zero); a0 = MFMA(sr1, P2[1][0], a0);
#pragma unroll
      for (int s = 0; s < 4; ++s) a0 = MFMA(xf[s], W1[s][0], a0);
      a0 = MFMA(ef, W3[0], a0);
    }
#define S0_ __builtin_bit_cast(bf16x8, __builtin_shufflevector(q00, q01, 0, 1, 2, 3, 4, 5, 6, 7))
#define S1_ __builtin_bit_cast(bf16x8, __builtin_shufflevector(q10, q11, 0, 1, 2, 3, 4, 5, 6, 7))
#define BF(v) __builtin_bit_cast(bf16x8, v)
#define PIN(lo, hi) _Pragma("unroll") for (int i = lo; i < hi; i += 4) asm volatile("" : "+v"(u[i]), "+v"(u[i + 1]), "+v"(u[i + 2]), "+v"(u[i + 3])); SB();
#define MULS(lo, hi) _Pragma("unroll") for (int i = lo; i < hi; ++i) u[i] = c[i] * -1.4426950408889634f; PIN(lo, hi)
#define EXPS(lo, hi) _Pragma("unroll") for (int i = lo; i < hi; ++i) u[i] = __builtin_amdgcn_exp2f(u[i]); PIN(lo, hi)
#define ADDS(lo, hi) _Pragma("unroll") for (int i = lo; i < hi; ++i) u[i] = 1.0f + u[i]; PIN(lo, hi)
#define RCPS(lo, hi) _Pragma("unroll") for (int i = lo; i < hi; ++i) u[i] = __builtin_amdgcn_rcpf(u[i]); PIN(lo, hi)
#define FINS(lo, hi) _Pragma("unroll") for (int i = lo; i < hi; ++i) u[i] *= c[i]; PIN(lo, hi)
    auto phase = [&](const int T, const int par, const int t, f32x16& c, f32x16& nx, f32x16& Yc, f32x16& Yp) {
      float u[16];
      SB();
      MULS(0, 8)
      Yp = MFMA(BF(mm1), S1_, Yp); SB();
      MULS(8, 16)
      if (T == 0) {
        q00 = __builtin_amdgcn_ds_read_tr16_b64_v4i16((LDS s16x4*)(lds + str0));
        q01 = __builtin_amdgcn_ds_read_tr16_b64_v4i16((LDS s16x4*)(lds + str1));
        q10 = __builtin_amdgcn_ds_read_tr16_b64_v4i16((LDS s16x4*)(lds + str0 + 1024));
        q11 = __builtin_amdgcn_ds_read_tr16_b64_v4i16((LDS s16x4*)(lds + str1 + 1024));
        asm volatile("" ::: "memory");
        *(LDS unsigned short*)(lds + sa) = 0;
        sa = (Bq[par ^ 1] >> 26) ^ skey;
        SB();
      } else {
        dma_tile(par ^ 1, t + 3, A4, Bq[par ^ 1]);
        SB();
      }
      const int U = T ^ 1;
      nx = MFMA(sr0, P2[0][U], zero); SB();
      EXPS(0, 4)
      nx = MFMA(sr1, P2[1][U], nx); SB();
      EXPS(4, 8)
      nx = MFMA(xf[0], W1[0][U], nx); SB();
      EXPS(8, 12)
      nx = MFMA(xf[1], W1[1][U], nx); SB();
      EXPS(12, 16)
      nx = MFMA(xf[2], W1[2][U], nx); SB();
      ADDS(0, 16)
      nx = MFMA(xf[3], W1[3][U], nx); SB();
      RCPS(0, 4)
      nx = MFMA(ef, W3[U], nx); SB();
      if (T == 0) {
        wait_dma(t + 2);
        *(LDS unsigned short*)(lds + sa) = 0x3F80;
        asm volatile("" ::: "memory");
        rd_frags(par ^ 1);
        A4 = *(LDS u32x4*)(lds + aoff + (t + 3) * 128);
        Bq[par ^ 1] = *(LDS unsigned*)(lds + boff + (t + 3) * 128);
        const int left = n - wb - 32 * t;
        *(LDS int*)(lds + pa) = left; *(LDS int*)(lds + pa + 2048) = left;
        oth = *(LDS int*)(lds + pa + 1024);
        SB();
      }
      if (T == 1) {
        const int o = __builtin_amdgcn_readfirstlane(oth), left = __builtin_amdgcn_readfirstlane(n - wb - 32 * t);
        asm volatile("s_cmp_ge_i32 %0, %1\n\ts_cbranch_scc1 1f\n\ts_setprio 0\n\ts_branch 2f\n\t1:\n\ts_setprio 1\n\t2:" ::"s"(left), "s"(o) : "scc");
        SB();
      }
      RCPS(4, 16)
      FINS(0, 16)
      mm0 = u32x4{pk(u[0], u[1]), pk(u[2], u[3]), pk(u[4], u[5]), pk(u[6], u[7])}; SB();
      mm1 = u32x4{pk(u[8], u[9]), pk(u[10], u[11]), pk(u[12], u[13]), pk(u[14], u[15])}; SB();
      Yc = MFMA(BF(mm0), S0_, Yc); SB();
    };
    for (int t = 0; t < nt; t += 2) {
      phase(0, 0, t, a0, a1, Y0, Y1);
      phase(1, 0, t, a1, a0, Y1, Y0);
      if (t + 1 >= nt) break;
      phase(0, 1, t + 1, a0, a1, Y0, Y1);
      phase(1, 1, t + 1, a1, a0, Y1, Y0);
    }
    Y1 = MFMA(BF(mm1), S1_, Y1);
    *(LDS unsigned short*)(lds + sa) = 0;
    wb += CAP;
    if (wb >= n) break;
    *(LDS u32x4*)(lds + lb + (tid & 63) * 16) = u32x4{512, 512, 512, 512};
    *(LDS u32x4*)(lds + lb + 1024 + (tid & 63) * 16) = u32x4{512, 512, 512, 512};
    asm volatile("" ::: "memory");
    window_scan();
    window_gather(true);
    window_lists();
  }

  STAMP(5); STAMPC(1, __builtin_amdgcn_s_memtime());
  f32x16 o0, o1;
#pragma unroll
  for (int i = 0; i < 16; ++i) { o0[i] = 0.f; o1[i] = 0.f; }
  auto ufrag = [&](int f) { return *(LDS bf16x8*)(lds + L_WU + f * 1024 + l * 16); };
#pragma unroll
  for (int s = 0; s < 4; ++s) { o0 = MFMA(ufrag(2 * s), XO[s], o0); o1 = MFMA(ufrag(2 * s + 1), XO[s], o1); }
#pragma unroll
  for (int s = 0; s < 2; ++s) {
    const bf16x8 z0 = __builtin_bit_cast(bf16x8, u32x4{pk(Y0[8 * s], Y0[8 * s + 1]), pk(Y0[8 * s + 2], Y0[8 * s + 3]), pk(Y0[8 * s + 4], Y0[8 * s + 5]), pk(Y0[8 * s + 6], Y0[8 * s + 7])});
    const bf16x8 z1 = __builtin_bit_cast(bf16x8, u32x4{pk(Y1[8 * s], Y1[8 * s + 1]), pk(Y1[8 * s + 2], Y1[8 * s + 3]), pk(Y1[8 * s + 4], Y1[8 * s + 5]), pk(Y1[8 * s + 6], Y1[8 * s + 7])});
    o0 = MFMA(ufrag(8 + 2 * s), z0, o0); o1 = MFMA(ufrag(9 + 2 * s), z0, o1);
    o0 = MFMA(ufrag(12 + 2 * s), z1, o0); o1 = MFMA(ufrag(13 + 2 * s), z1, o1);
  }
#pragma unroll
  for (int q = 0; q < 4; ++q) {
    const u32x2 v0 = {pk(silu1(o0[4 * q]), silu1(o0[4 * q + 1])), pk(silu1(o0[4 * q + 2]), silu1(o0[4 * q + 3]))};
    const u32x2 v1 = {pk(silu1(o1[4 * q]), silu1(o1[4 * q + 1])), pk(silu1(o1[4 * q + 2]), silu1(o1[4 * q + 3]))};
    *(LDS u32x2*)(lds + stg + r * 128 + ((q ^ (r & 7)) << 4) + h * 8) = v0;
    *(LDS u32x2*)(lds + stg + r * 128 + (((4 + q) ^ (r & 7)) << 4) + h * 8) = v1;
  }
  asm volatile("" ::: "memory");
#pragma unroll
  for (int it = 0; it < 4; ++it) {
    const int m = l + 64 * it, row = m >> 3, p = m & 7;
    if (m < GP * 8) {
      const u32x4 v = *(LDS u32x4*)(lds + stg + row * 128 + ((p ^ (row & 7)) << 4));
      *(u32x4*)(out + (size_t)(GP * g) * 128 + m * 16) = v;
    }
  }
  STAMP(6);
}

static inline void launch_part(void* const* d_in, void* d_ws, hipStream_t stream) {
  k_part<<<NC + 5, 512, 0, stream>>>((const i32x4*)d_in[3], (const i32x4*)d_in[2], (const u32x4*)d_in[0], (const uint16_t*)d_in[4], (const uint16_t*)d_in[5], (char*)d_ws);
}
static inline void launch_main(void* const* d_in, void* d_out, void* d_ws, hipStream_t stream) {
  k_main<<<NB, 512, 0, stream>>>((const char*)d_in[0], (const char*)d_in[1], (const char*)d_ws, (char*)d_out);
}

extern "C" void kernel_launch(void* const* d_in, const int* in_sizes, int n_in, void* d_out, int out_size, void* d_ws, size_t ws_size,
                              hipStream_t stream) {
  launch_part(d_in, d_ws, stream);
  launch_main(d_in, d_out, d_ws, stream);
}
```

```cpp
#include <hip/hip_runtime.h>
#include <stdint.h>

#define LDS __attribute__((address_space(3)))
#define DI __device__ __forceinline__
typedef float f32x16 __attribute__((ext_vector_type(16)));
typedef float f32x2 __attribute__((ext_vector_type(2)));
typedef unsigned u32x4 __attribute__((ext_vector_type(4)));
typedef unsigned u32x2 __attribute__((ext_vector_type(2)));
typedef int i32x4 __attribute__((ext_vector_type(4)));
typedef __bf16 bf16x8 __attribute__((ext_vector_type(8)));
typedef __bf16 bf16x2 __attribute__((ext_vector_type(2)));
typedef short s16x4 __attribute__((ext_vector_type(4)));

constexpr int NN = 50000;
constexpr int NE = 800000;
constexpr int GP = 25;
constexpr int CE = 3200;
constexpr int NC = 250;
constexpr int NB = 256;
constexpr int TROW = 4096;
constexpr int CAP = 512;
constexpr unsigned WS_EP = 0, WS_META = 25600000u, WS_OFF = 28800000u, WS_WF = 29824000u;

#define MFMA(a, b, c) __builtin_amdgcn_mfma_f32_32x32x16_bf16((a), (b), (c), 0, 0, 0)
#define BAR() asm volatile("s_waitcnt lgkmcnt(0)\n\ts_barrier" ::: "memory")
#define SB() __builtin_amdgcn_sched_barrier(0)
#define EPOL "nt"
#define STAMP(k)
#define STAMPC(k, v)

DI unsigned pk(float a, float b) { f32x2 v = {a, b}; bf16x2 r = __builtin_convertvector(v, bf16x2); return __builtin_bit_cast(unsigned, r); }
DI unsigned wave_incl_scan(unsigned x) {
  x += __builtin_amdgcn_update_dpp(0u, x, 0x111, 0xf, 0xf, false);
  x += __builtin_amdgcn_update_dpp(0u, x, 0x112, 0xf, 0xf, false);
  x += __builtin_amdgcn_update_dpp(0u, x, 0x114, 0xf, 0xf, false);
  x += __builtin_amdgcn_update_dpp(0u, x, 0x118, 0xf, 0xf, false);
  x += __builtin_amdgcn_update_dpp(0u, x, 0x142, 0xa, 0xf, false);
  x += __builtin_amdgcn_update_dpp(0u, x, 0x143, 0xc, 0xf, false);
  return x;
}

__global__ __launch_bounds__(512) void k_part(const i32x4* __restrict__ dst4, const i32x4* __restrict__ src4, const u32x4* __restrict__ x16,
                                              const uint16_t* __restrict__ wm, const uint16_t* __restrict__ wu, char* __restrict__ ws) {
  __shared__ __attribute__((aligned(16))) unsigned cnt[2048];
  __shared__ __attribute__((aligned(16))) unsigned metaL[CE];
  __shared__ unsigned wsum[8];
  const int tid = threadIdx.x, c = blockIdx.x;
#undef PROFBUF
#define PROFBUF (ws + (8u << 20))
  if (c >= NC) {
    const int it = (c - NC) * 512 + tid;
    if (it < 34 * 64) {
      const int f = it >> 6, l = it & 63, col = l & 31, h = l >> 5;
      unsigned short v[8];
#pragma unroll
      for (int j = 0; j < 8; ++j) {
        if (f < 18) {
          const int t = f & 1, s = (f >> 1) & 3;
          const int row = (f < 8 ? 0 : (f < 16 ? 64 : 128)) + (f < 16 ? 16 * s : 0) + 8 * h + j;
          v[j] = wm[row * 64 + 32 * t + col];
        } else {
          const int ks = (f - 18) >> 1, u = f & 1;
          const int row = ks < 4 ? 16 * ks + 8 * h + j : 64 + 32 * ((ks - 4) >> 1) + 16 * (ks & 1) + 8 * (j >> 2) + 4 * h + (j & 3);
          v[j] = wu[row * 64 + 32 * u + col];
        }
      }
      u32x4 o = {v[0] | ((unsigned)v[1] << 16), v[2] | ((unsigned)v[3] << 16), v[4] | ((unsigned)v[5] << 16), v[6] | ((unsigned)v[7] << 16)};
      if (f < 18) {
#pragma unroll
        for (int q = 0; q < 4; ++q)
          o[q] = pk(__builtin_bit_cast(float, o[q] << 16) * -1.4426950408889634f, __builtin_bit_cast(float, o[q] & 0xFFFF0000u) * -1.4426950408889634f);
      }
      *(u32x4*)(ws + WS_WF + it * 16) = o;
    }
    return;
  }
  STAMP(0);
  constexpr int TA = CE / 8;
  i32x4 d[2], s[2];
  const int ta = tid < TA ? tid : TA - 1;
#pragma unroll
  for (int i = 0; i < 2; ++i) { d[i] = dst4[c * (CE / 4) + ta + i * TA]; s[i] = src4[c * (CE / 4) + ta + i * TA]; }
  *(u32x4*)&cnt[4 * tid] = u32x4{0, 0, 0, 0};
  BAR();
  STAMP(1);
  unsigned g[8], rk[8];
  if (tid < TA) {
#pragma unroll
    for (int j = 0; j < 8; ++j) { g[j] = ((unsigned)d[j >> 2][j & 3] * 128u + 127u) / 3125u; rk[j] = __hip_atomic_fetch_add(&cnt[g[j]], 1u, __ATOMIC_RELAXED, __HIP_MEMORY_SCOPE_WORKGROUP); }
  }
  BAR();
  STAMP(2);
  const u32x4 cc = *(const u32x4*)&cnt[4 * tid];
  const unsigned tot = cc[0] + cc[1] + cc[2] + cc[3];
  const unsigned incl = wave_incl_scan(tot);
  if ((tid & 63) == 63) wsum[tid >> 6] = incl;
  BAR();
  unsigned ws8 = wsum[tid & 7];
  ws8 += __builtin_amdgcn_update_dpp(0u, ws8, 0x111, 0xf, 0xf, false);
  ws8 += __builtin_amdgcn_update_dpp(0u, ws8, 0x112, 0xf, 0xf, false);
  ws8 += __builtin_amdgcn_update_dpp(0u, ws8, 0x114, 0xf, 0xf, false);
  const int w = __builtin_amdgcn_readfirstlane(tid >> 6);
  const unsigned wb = w ? __builtin_amdgcn_readlane(ws8, w - 1) : 0u;
  const unsigned o0 = wb + incl - tot, o1 = o0 + cc[0], o2 = o1 + cc[1], o3 = o2 + cc[2];
  *(u32x4*)&cnt[4 * tid] = u32x4{o0, o1, o2, o3};
  *(u32x2*)(ws + WS_OFF + c * TROW + tid * 8) = u32x2{o0 | (o1 << 16), o2 | (o3 << 16)};
  BAR();
  STAMP(3);
  if (tid < TA) {
#pragma unroll
    for (int j = 0; j < 8; ++j) {
      const unsigned dv = d[j >> 2][j & 3], sv = s[j >> 2][j & 3];
      metaL[cnt[g[j]] + rk[j]] = sv * (GP * CE) + (dv - (__umul24(g[j], 3125u) >> 7)) * CE + (4 * tid + (j & 3) + (j >> 2) * (4 * TA));
    }
  }
  BAR();
  STAMP(4);
  if (tid < TA) {
#pragma unroll
    for (int i = 0; i < 2; ++i) *(u32x4*)(ws + WS_META + c * (CE * 4) + (tid + i * TA) * 16) = *(const u32x4*)&metaL[4 * (tid + i * TA)];
  }
  STAMP(5);
  STAMP(6);
}

constexpr unsigned L_STG = 0;
constexpr unsigned SLOT = 40960;
constexpr unsigned L_S = 81920;
constexpr unsigned L_LIST = 98304;
constexpr unsigned L_WU = 131072;
constexpr unsigned L_T1 = 147456;
constexpr unsigned L_T2 = 151552;
constexpr unsigned L_PRG = 155648;
constexpr unsigned L_END = 158720;
constexpr unsigned L_M = 0;
constexpr unsigned L_OWN = 40960;
constexpr unsigned L_WIMG = 73728;

DI void dma16(unsigned m0, unsigned voff, __amdgpu_buffer_rsrc_t srd, unsigned soff) {
  asm volatile("s_mov_b32 m0, %0\n\ts_nop 0\n\tbuffer_load_dwordx4 %1, %2, %3 offen lds" ::"s"(m0), "v"(voff), "s"(srd), "s"(soff) : "m0", "memory");
}
DI unsigned umax(unsigned a, unsigned b) { return a > b ? a : b; }
DI unsigned wave_excl_max(unsigned x) {
  x = umax(x, __builtin_amdgcn_update_dpp(0u, x, 0x111, 0xf, 0xf, false));
  x = umax(x, __builtin_amdgcn_update_dpp(0u, x, 0x112, 0xf, 0xf, false));
  x = umax(x, __builtin_amdgcn_update_dpp(0u, x, 0x114, 0xf, 0xf, false));
  x = umax(x, __builtin_amdgcn_update_dpp(0u, x, 0x118, 0xf, 0xf, false));
  x = umax(x, __builtin_amdgcn_update_dpp(0u, x, 0x142, 0xa, 0xf, false));
  x = umax(x, __builtin_amdgcn_update_dpp(0u, x, 0x143, 0xc, 0xf, false));
  return __builtin_amdgcn_update_dpp(0u, x, 0x138, 0xf, 0xf, false);
}
DI float silu1(float v) { return v * __builtin_amdgcn_rcpf(1.0f + __builtin_amdgcn_exp2f(v * -1.4426950408889634f)); }

__global__ __launch_bounds__(512, 2) void k_main(const char* __restrict__ x, const char* __restrict__ ea, const char* __restrict__ ws, char* __restrict__ out) {
  __shared__ __attribute__((aligned(1024))) char lds_[L_END];
  LDS char* lds = (LDS char*)lds_;
  const unsigned ldsb = (unsigned)(size_t)lds;
  const int tid = threadIdx.x, l = tid & 63, w = __builtin_amdgcn_readfirstlane(tid >> 6);
  const int xcd = blockIdx.x & 7, b = xcd * 32 + (blockIdx.x >> 3);
  const int g = 8 * b + w, nbase = (g * 3125) >> 7, ncnt = (((g + 1) * 3125) >> 7) - nbase;
  const int r = l & 31, h = l >> 5;
#undef PROFBUF
#define PROFBUF ((char*)ws)
  STAMP(0);
  const auto sx = __builtin_amdgcn_make_buffer_rsrc((void*)x, 0, NN * 128, 0x00020000);
  const auto se = __builtin_amdgcn_make_buffer_rsrc((void*)ea, 0, NE * 32, 0x00020000);
  const auto sm = __builtin_amdgcn_make_buffer_rsrc((void*)(ws + WS_META), 0, NE * 4, 0x00020000);
  const auto sw = __builtin_amdgcn_make_buffer_rsrc((void*)(ws + WS_WF), 0, 34 * 1024, 0x00020000);

  const auto st = __builtin_amdgcn_make_buffer_rsrc((void*)(ws + WS_OFF), 0, NC * TROW, 0x00020000);

  const unsigned stg = L_STG + w * 5120;
  asm volatile("s_nop 4" ::: "memory");
  dma16(ldsb + L_T1 + w * 1024, (tid & 255) * TROW + b * 16 + (w >> 2) * 16, st, 0);
  const unsigned sreg = L_S + w * 2048;
  const unsigned la = L_LIST + w * 4096, lb = la + 2048;
  *(LDS u32x4*)(lds + lb + l * 16) = u32x4{512, 512, 512, 512};
  *(LDS u32x4*)(lds + lb + 1024 + l * 16) = u32x4{512, 512, 512, 512};
  f32x16 Y0, Y1;
#pragma unroll
  for (int i = 0; i < 16; ++i) { Y0[i] = 0.f; Y1[i] = 0.f; }

  unsigned xch[4];
#pragma unroll
  for (int s = 0; s < 4; ++s) xch[s] = ((l & 7) ^ (((8 * s + (l >> 3)) >> 1) & 7)) << 4;
  unsigned ech = ((l & 1) ^ ((l >> 4) & 1)) << 4;
  unsigned skey = sreg + (l >> 1) * 64 + (((l >> 3) & 3) << 4);
  unsigned aoff = la + (l >> 3) * 16;
  unsigned boff = lb + (4 * ((l >> 1) & 7) + (l >> 4)) * 4;
  unsigned xrd[4];
#pragma unroll
  for (int s = 0; s < 4; ++s) xrd[s] = stg + r * 128 + (((2 * s + h) ^ ((r >> 1) & 7)) << 4);
  unsigned erd = stg + 4096 + r * 32 + ((h ^ ((r >> 3) & 1)) << 4);
  unsigned srow0 = sreg + r * 64 + ((h ^ ((r >> 2) & 3)) << 4), srow1 = srow0 ^ 32;
  unsigned str0 = sreg + (4 * h + ((l & 15) >> 2)) * 64 + (((2 * ((l >> 4) & 1) + ((l & 3) >> 1)) ^ h) << 4) + (l & 1) * 8;
  unsigned str1 = (str0 ^ 32) + 512;

  asm volatile("" : "+v"(xch[0]), "+v"(xch[1]), "+v"(ech), "+v"(skey), "+v"(aoff), "+v"(boff), "+v"(xrd[0]), "+v"(xrd[1]), "+v"(xrd[2]), "+v"(xrd[3]));
  asm volatile("" : "+v"(erd), "+v"(srow0), "+v"(srow1), "+v"(str0), "+v"(str1), "+v"(Y0), "+v"(Y1));
  xch[2] = xch[0]; xch[3] = xch[1];
  asm volatile("s_waitcnt vmcnt(0)" ::: "memory");
  if (b == NB - 1 && w >= 4) *(LDS unsigned short*)(lds + L_T2 + (tid & 255) * 16) = (tid & 255) < NC ? CE : 0;
  BAR();
  STAMP(1);
  {
    const unsigned c0 = 32 * w + (l >> 3);
    unsigned s0[4], s1[4], vo[4];
#pragma unroll
    for (int i = 0; i < 4; ++i) { s0[i] = *(LDS unsigned short*)(lds + L_T1 + c0 * 16 + i * 128); s1[i] = *(LDS unsigned short*)(lds + L_T2 + c0 * 16 + i * 128); }
#pragma unroll
    for (int i = 0; i < 4; ++i) {
      const unsigned o = (s0[i] & ~3u) + 4 * (l & 7);
      vo[i] = o < s1[i] ? (__umul24(c0 + 8 * i, CE) + o) * 4 : 0x7FFFFFF0u;
    }
#pragma unroll
    for (int i = 0; i < 4; ++i) dma16(ldsb + L_M + (32 * w + 8 * i) * 128, vo[i], sm, 0);
  }

  int wb = 0, n, nw;
  unsigned cj[8], mt[8];
  auto window_scan = [&]() {
    int l = tid & 63;
    asm volatile("" : "+v"(l));
    unsigned s0[4], o0[4], o1[4], cn[4], rb[4], ix[4];
    const unsigned ta = L_T1 + l * 16, tb = ta + w * 2, tc = l * 16 + (w < 7 ? L_T1 + w * 2 + 2 : L_T2);
#pragma unroll
    for (int k = 0; k < 4; ++k) {
      s0[k] = *(LDS unsigned short*)(lds + ta + k * 1024);
      o0[k] = *(LDS unsigned short*)(lds + tb + k * 1024);
      o1[k] = *(LDS unsigned short*)(lds + tc + k * 1024);
    }
    unsigned tot = 0;
#pragma unroll
    for (int k = 0; k < 4; ++k) {
      cn[k] = o1[k] - o0[k];
      if (k == 3) cn[k] = l < NC - 192 ? cn[k] : 0u;
      rb[k] = (l << 13) + o0[k] - (s0[k] & ~3u) + ((k << 19) + 512 + (1 << 21));
      ix[k] = tot;
      tot += cn[k];
    }
    const unsigned incl = wave_incl_scan(tot);
    n = __builtin_amdgcn_readlane(incl, 63);
    nw = n - wb < CAP ? n - wb : CAP;
    const int base = (int)(incl - tot) - wb;
#pragma unroll
    for (int k = 0; k < 4; ++k) {
      const int lo = base + (int)ix[k];
      const int pos = lo > 0 ? lo : 0;
      const bool on = pos < lo + (int)cn[k] && pos < CAP;
      *(LDS unsigned*)(lds + (on ? lb + pos * 4 : la + l * 4)) = (pos << 21) + (rb[k] - lo);
    }
    asm volatile("" ::: "memory");
    const u32x4 m0 = *(LDS u32x4*)(lds + lb + l * 32), m1 = *(LDS u32x4*)(lds + lb + l * 32 + 16);
    unsigned run[8];
    run[0] = m0[0];
#pragma unroll
    for (int j = 1; j < 8; ++j) run[j] = umax(run[j - 1], j < 4 ? m0[j] : m1[j - 4]);
    const unsigned ex = wave_excl_max(run[7]);
    const int last = (nw > 0 ? nw - 1 : 0) - 512, i0 = 8 * l - 512;
#pragma unroll
    for (int j = 0; j < 8; ++j) {
      const int im = i0 < last - j ? i0 : last - j;
      cj[j] = (umax(run[j], ex) & 0x1FFFFFu) + im + j;
    }
  };
  auto window_gather = [&](bool all) {
#pragma unroll
    for (int j = 0; j < 8; ++j) {
      const unsigned c = cj[j] >> 13, q = cj[j] & 0x1FFFu;
      const unsigned s0 = *(LDS unsigned short*)(lds + L_T1 + c * 16) & ~3u;
      const unsigned v = __builtin_amdgcn_raw_buffer_load_b32(sm, (__umul24(c, CE) + s0 + q) * 4, 0, 0);
      mt[j] = (all || q >= 32) ? v : mt[j];
    }
  };
  window_scan();
  unsigned pa = L_PRG + w * 256 + l * 4;
  asm volatile("" : "+v"(pa));
  *(LDS int*)(lds + pa) = n; *(LDS int*)(lds + pa + 2048) = n;
#pragma unroll
  for (int i = 0; i < 5; ++i) {
    const int f = w + 8 * i;
    const unsigned dstl = f < 18 ? L_WIMG + f * 1024 : L_WU + (f - 18) * 1024;
    if (f < 34) dma16(ldsb + dstl, l * 16, sw, f * 1024);
  }
  const unsigned own = L_OWN + w * 4096;
#pragma unroll
  for (int s = 0; s < 4; ++s) {
    const int R = 8 * s + (l >> 3);
    int node = nbase + R; node = node < NN ? node : NN - 1;
    dma16(ldsb + own + s * 1024, node * 128 + (((l & 7) ^ ((R >> 1) & 7)) << 4), sx, 0);
  }

  STAMP(2);
  asm volatile("s_waitcnt vmcnt(8)" ::: "memory");
  BAR();
  {
    unsigned acc = 0;
#pragma unroll
    for (int j = 0; j < 8; ++j) {
      acc |= cj[j];
      mt[j] = *(LDS unsigned*)(lds + L_M + (((cj[j] >> 13) << 5) + (cj[j] & 31)) * 4);
    }
    const bool over = (acc & 0x1FE0u) != 0;
    BAR();
    if (__any(over)) window_gather(false);
  }
  int nt;
  u32x4 A4;
  unsigned Bq[2];
  auto dma_tile = [&](const int slot, const int tt, const u32x4 A, const unsigned B) {
    const unsigned v0 = A[0] | xch[0], v1 = A[1] | xch[1], v2 = A[2] | xch[2], v3 = A[3] | xch[3], v4 = (B & 0x07FFFFFFu) | ech;
    asm volatile("s_waitcnt lgkmcnt(0)\n\t"
                 "s_cmp_lt_i32 %[t], %[nt]\n\ts_cbranch_scc0 1f\n\t"
                 "s_mov_b32 m0, %[m]\n\ts_nop 0\n\tbuffer_load_dwordx4 %[v0], %[sx], 0 offen lds\n\t"
                 "s_add_u32 m0, %[m], 0x400\n\ts_nop 0\n\tbuffer_load_dwordx4 %[v1], %[sx], 0 offen lds\n\t"
                 "s_add_u32 m0, %[m], 0x800\n\ts_nop 0\n\tbuffer_load_dwordx4 %[v2], %[sx], 0 offen lds\n\t"
                 "s_add_u32 m0, %[m], 0xc00\n\ts_nop 0\n\tbuffer_load_dwordx4 %[v3], %[sx], 0 offen lds\n\t"
                 "s_add_u32 m0, %[m], 0x1000\n\ts_nop 0\n\tbuffer_load_dwordx4 %[v4], %[se], 0 offen " EPOL " lds\n\t"
                 "1:"
                 ::[t] "s"(tt), [nt] "s"(nt), [m] "s"(ldsb + stg + slot * SLOT), [v0] "v"(v0), [v1] "v"(v1), [v2] "v"(v2), [v3] "v"(v3), [v4] "v"(v4), [sx] "s"(sx), [se] "s"(se)
                 : "m0", "scc", "memory");
  };
  auto wait_dma = [&](const int tt) {
    asm volatile("s_cmp_lt_i32 %0, %1\n\ts_cbranch_scc1 1f\n\ts_waitcnt vmcnt(0)\n\t1:\n\ts_waitcnt vmcnt(5)" ::"s"(tt), "s"(nt) : "scc", "memory");
  };
  auto window_lists = [&]() {
    nt = (nw + 31) >> 5;
    {
      int l = tid & 63;
      asm volatile("" : "+v"(l));
      unsigned av[8], bv[8];
#pragma unroll
      for (int j = 0; j < 8; ++j) {
        const unsigned t = mt[j] >> 7, sn = __umulhi(t, 54975582u) >> 3;
        const unsigned u = (unsigned)__mul24((int)sn, -625) + t;
        const unsigned dl = __umul24(u, 41u) >> 10;
        const unsigned eh = (unsigned)__mul24((int)dl, -25) + __umul24(cj[j] >> 13, 25u) + u;
        av[j] = sn << 7;
        bv[j] = (eh << 12) | ((mt[j] << 5) & 0xFE0u) | ((8 * l + j < nw ? dl : 31u) << 27);
      }
      *(LDS u32x4*)(lds + la + l * 32) = u32x4{av[0], av[1], av[2], av[3]};
      *(LDS u32x4*)(lds + la + l * 32 + 16) = u32x4{av[4], av[5], av[6], av[7]};
      *(LDS u32x4*)(lds + lb + l * 32) = u32x4{bv[0], bv[1], bv[2], bv[3]};
      *(LDS u32x4*)(lds + lb + l * 32 + 16) = u32x4{bv[4], bv[5], bv[6], bv[7]};
      asm volatile("" ::: "memory");
    }
    A4 = *(LDS u32x4*)(lds + aoff); Bq[0] = *(LDS unsigned*)(lds + boff);
    dma_tile(0, 0, A4, Bq[0]);
  };
  window_lists();
  wait_dma(0);
  BAR();
  STAMP(3);

  bf16x8 W1[4][2], W3[2], P2[2][2], XO[4];
  {
    auto wfrag = [&](int f) { return *(LDS bf16x8*)(lds + L_WIMG + f * 1024 + l * 16); };
#pragma unroll
    for (int s = 0; s < 4; ++s) { W1[s][0] = wfrag(2 * s); W1[s][1] = wfrag(2 * s + 1); }
    W3[0] = wfrag(16); W3[1] = wfrag(17);
    const int rs = (r & 19) | ((r & 4) << 1) | ((r & 8) >> 1);
    f32x16 p0, p1;
#pragma unroll
    for (int i = 0; i < 16; ++i) { p0[i] = 0.f; p1[i] = 0.f; }
#pragma unroll
    for (int s = 0; s < 4; ++s) {
      const bf16x8 a = *(LDS bf16x8*)(lds + own + rs * 128 + (((2 * s + h) ^ ((rs >> 1) & 7)) << 4));
      p0 = MFMA(a, wfrag(8 + 2 * s), p0);
      p1 = MFMA(a, wfrag(9 + 2 * s), p1);
      XO[s] = *(LDS bf16x8*)(lds + own + r * 128 + (((2 * s + h) ^ ((r >> 1) & 7)) << 4));
    }
#pragma unroll
    for (int s = 0; s < 2; ++s) {
      P2[s][0] = __builtin_bit_cast(bf16x8, u32x4{pk(p0[8 * s], p0[8 * s + 1]), pk(p0[8 * s + 2], p0[8 * s + 3]), pk(p0[8 * s + 4], p0[8 * s + 5]), pk(p0[8 * s + 6], p0[8 * s + 7])});
      P2[s][1] = __builtin_bit_cast(bf16x8, u32x4{pk(p1[8 * s], p1[8 * s + 1]), pk(p1[8 * s + 2], p1[8 * s + 3]), pk(p1[8 * s + 4], p1[8 * s + 5]), pk(p1[8 * s + 6], p1[8 * s + 7])});
    }
  }
  BAR();
  *(LDS u32x4*)(lds + sreg + l * 16) = u32x4{0, 0, 0, 0};
  *(LDS u32x4*)(lds + sreg + 1024 + l * 16) = u32x4{0, 0, 0, 0};
  while (true) {
    STAMP(4); STAMPC(0, __builtin_amdgcn_s_memtime()); STAMPC(2, nt);
    f32x16 a0, a1;
    bf16x8 xf[4], ef, sr0, sr1;
    s16x4 q00 = {0, 0, 0, 0}, q01 = q00, q10 = q00, q11 = q00;
    u32x4 mm0 = {0, 0, 0, 0}, mm1 = mm0;
    unsigned sa;
    int oth, mine;
    f32x16 zero;
#pragma unroll
    for (int i = 0; i < 16; ++i) zero[i] = 0.f;
    auto rd_frags = [&](const int slot) {
#pragma unroll
      for (int s = 0; s < 4; ++s) xf[s] = *(LDS bf16x8*)(lds + xrd[s] + slot * SLOT);
      ef = *(LDS bf16x8*)(lds + erd + slot * SLOT);
      sr0 = *(LDS bf16x8*)(lds + srow0); sr1 = *(LDS bf16x8*)(lds + srow1);
    };
    asm volatile("" ::: "memory");
    {
      const u32x4 A1 = *(LDS u32x4*)(lds + aoff + 128); Bq[1] = *(LDS unsigned*)(lds + boff + 128);
      dma_tile(1, 1, A1, Bq[1]);
      wait_dma(1);
      sa = (Bq[0] >> 26) ^ skey;
      *(LDS unsigned short*)(lds + sa) = 0x3F80;
      asm volatile("" ::: "memory");
      rd_frags(0);
      A4 = *(LDS u32x4*)(lds + aoff + 256); Bq[0] = *(LDS unsigned*)(lds + boff + 256);
      dma_tile(0, 2, A4, Bq[0]);
      a0 = MFMA(sr0, P2[0][0], zero); a0 = MFMA(sr1, P2[1][0], a0);
#pragma unroll
      for (int s = 0; s < 4; ++s) a0 = MFMA(xf[s], W1[s][0], a0);
      a0 = MFMA(ef, W3[0], a0);
    }
#define S0_ __builtin_bit_cast(bf16x8, __builtin_shufflevector(q00, q01, 0, 1, 2, 3, 4, 5, 6, 7))
#define S1_ __builtin_bit_cast(bf16x8, __builtin_shufflevector(q10, q11, 0, 1, 2, 3, 4, 5, 6, 7))
#define BF(v) __builtin_bit_cast(bf16x8, v)
#define PIN(lo, hi) _Pragma("unroll") for (int i = lo; i < hi; i += 4) asm volatile("" : "+v"(u[i]), "+v"(u[i + 1]), "+v"(u[i + 2]), "+v"(u[i + 3])); SB();
#define EXPS(lo, hi) _Pragma("unroll") for (int i = lo; i < hi; ++i) u[i] = __builtin_amdgcn_exp2f(c[i]); PIN(lo, hi)
#define ADDS(lo, hi) _Pragma("unroll") for (int i = lo; i < hi; ++i) u[i] = 1.0f + u[i]; PIN(lo, hi)
#define RCPS(lo, hi) _Pragma("unroll") for (int i = lo; i < hi; ++i) u[i] = __builtin_amdgcn_rcpf(u[i]); PIN(lo, hi)
#define FINS(lo, hi) _Pragma("unroll") for (int i = lo; i < hi; ++i) u[i] *= c[i]; PIN(lo, hi)
    auto phase = [&](const int T, const int par, const int t, f32x16& c, f32x16& nx, f32x16& Yc, f32x16& Yp) {
      float u[16];
      SB();
      Yp = MFMA(BF(mm1), S1_, Yp); SB();
      if (T == 0) {
        q00 = __builtin_amdgcn_ds_read_tr16_b64_v4i16((LDS s16x4*)(lds + str0));
        q01 = __builtin_amdgcn_ds_read_tr16_b64_v4i16((LDS s16x4*)(lds + str1));
        q10 = __builtin_amdgcn_ds_read_tr16_b64_v4i16((LDS s16x4*)(lds + str0 + 1024));
        q11 = __builtin_amdgcn_ds_read_tr16_b64_v4i16((LDS s16x4*)(lds + str1 + 1024));
        asm volatile("" ::: "memory");
        *(LDS unsigned short*)(lds + sa) = 0;
        sa = (Bq[par ^ 1] >> 26) ^ skey;
        SB();
      } else {
        dma_tile(par ^ 1, t + 3, A4, Bq[par ^ 1]);
        SB();
      }
      const int U = T ^ 1;
      nx = MFMA(sr0, P2[0][U], zero); SB();
      EXPS(0, 4)
      nx = MFMA(sr1, P2[1][U], nx); SB();
      EXPS(4, 8)
      nx = MFMA(xf[0], W1[0][U], nx); SB();
      EXPS(8, 12)
      nx = MFMA(xf[1], W1[1][U], nx); SB();
      EXPS(12, 16)
      nx = MFMA(xf[2], W1[2][U], nx); SB();
      ADDS(0, 16)
      nx = MFMA(xf[3], W1[3][U], nx); SB();
      RCPS(0, 4)
      nx = MFMA(ef, W3[U], nx); SB();
      if (T == 0) {
        wait_dma(t + 2);
        *(LDS unsigned short*)(lds + sa) = 0x3F80;
        asm volatile("" ::: "memory");
        rd_frags(par ^ 1);
        A4 = *(LDS u32x4*)(lds + aoff + (t + 3) * 128);
        Bq[par ^ 1] = *(LDS unsigned*)(lds + boff + (t + 3) * 128);
        const int left = n - wb - 32 * t;
        *(LDS int*)(lds + pa) = left; *(LDS int*)(lds + pa + 2048) = left;
        oth = *(LDS int*)(lds + pa + 1024);
        SB();
      }
      if (T == 1) {
        const int o = __builtin_amdgcn_readfirstlane(oth), left = __builtin_amdgcn_readfirstlane(n - wb - 32 * t);
        asm volatile("s_cmp_ge_i32 %0, %1\n\ts_cbranch_scc1 1f\n\ts_setprio 0\n\ts_branch 2f\n\t1:\n\ts_setprio 1\n\t2:" ::"s"(left), "s"(o) : "scc");
        SB();
      }
      RCPS(4, 16)
      FINS(0, 16)
      mm0 = u32x4{pk(u[0], u[1]), pk(u[2], u[3]), pk(u[4], u[5]), pk(u[6], u[7])}; SB();
      mm1 = u32x4{pk(u[8], u[9]), pk(u[10], u[11]), pk(u[12], u[13]), pk(u[14], u[15])}; SB();
      Yc = MFMA(BF(mm0), S0_, Yc); SB();
    };
    for (int t = 0; t < nt; t += 2) {
      phase(0, 0, t, a0, a1, Y0, Y1);
      phase(1, 0, t, a1, a0, Y1, Y0);
      if (t + 1 >= nt) break;
      phase(0, 1, t + 1, a0, a1, Y0, Y1);
      phase(1, 1, t + 1, a1, a0, Y1, Y0);
    }
    Y1 = MFMA(BF(mm1), S1_, Y1);
    *(LDS unsigned short*)(lds + sa) = 0;
    wb += CAP;
    if (wb >= n) break;
    *(LDS u32x4*)(lds + lb + (tid & 63) * 16) = u32x4{512, 512, 512, 512};
    *(LDS u32x4*)(lds + lb + 1024 + (tid & 63) * 16) = u32x4{512, 512, 512, 512};
    asm volatile("" ::: "memory");
    window_scan();
    window_gather(true);
    window_lists();
  }

  STAMP(5); STAMPC(1, __builtin_amdgcn_s_memtime());
#pragma unroll
  for (int i = 0; i < 16; ++i) { Y0[i] *= -0.6931471805599453f; Y1[i] *= -0.6931471805599453f; }
  f32x16 o0, o1;
#pragma unroll
  for (int i = 0; i < 16; ++i) { o0[i] = 0.f; o1[i] = 0.f; }
  auto ufrag = [&](int f) { return *(LDS bf16x8*)(lds + L_WU + f * 1024 + l * 16); };
#pragma unroll
  for (int s = 0; s < 4; ++s) { o0 = MFMA(ufrag(2 * s), XO[s], o0); o1 = MFMA(ufrag(2 * s + 1), XO[s], o1); }
#pragma unroll
  for (int s = 0; s < 2; ++s) {
    const bf16x8 z0 = __builtin_bit_cast(bf16x8, u32x4{pk(Y0[8 * s], Y0[8 * s + 1]), pk(Y0[8 * s + 2], Y0[8 * s + 3]), pk(Y0[8 * s + 4], Y0[8 * s + 5]), pk(Y0[8 * s + 6], Y0[8 * s + 7])});
    const bf16x8 z1 = __builtin_bit_cast(bf16x8, u32x4{pk(Y1[8 * s], Y1[8 * s + 1]), pk(Y1[8 * s + 2], Y1[8 * s + 3]), pk(Y1[8 * s + 4], Y1[8 * s + 5]), pk(Y1[8 * s + 6], Y1[8 * s + 7])});
    o0 = MFMA(ufrag(8 + 2 * s), z0, o0); o1 = MFMA(ufrag(9 + 2 * s), z0, o1);
    o0 = MFMA(ufrag(12 + 2 * s), z1, o0); o1 = MFMA(ufrag(13 + 2 * s), z1, o1);
  }
#pragma unroll
  for (int q = 0; q < 4; ++q) {
    const u32x2 v0 = {pk(silu1(o0[4 * q]), silu1(o0[4 * q + 1])), pk(silu1(o0[4 * q + 2]), silu1(o0[4 * q + 3]))};
    const u32x2 v1 = {pk(silu1(o1[4 * q]), silu1(o1[4 * q + 1])), pk(silu1(o1[4 * q + 2]), silu1(o1[4 * q + 3]))};
    *(LDS u32x2*)(lds + stg + r * 128 + ((q ^ (r & 7)) << 4) + h * 8) = v0;
    *(LDS u32x2*)(lds + stg + r * 128 + (((4 + q) ^ (r & 7)) << 4) + h * 8) = v1;
  }
  asm volatile("" ::: "memory");
#pragma unroll
  for (int it = 0; it < 4; ++it) {
    const int m = l + 64 * it, row = m >> 3, p = m & 7;
    if (m < ncnt * 8) {
      const u32x4 v = *(LDS u32x4*)(lds + stg + row * 128 + ((p ^ (row & 7)) << 4));
      *(u32x4*)(out + (size_t)nbase * 128 + m * 16) = v;
    }
  }
  STAMP(6);
}

static inline void launch_part(void* const* d_in, void* d_ws, hipStream_t stream) {
  k_part<<<NC + 5, 512, 0, stream>>>((const i32x4*)d_in[3], (const i32x4*)d_in[2], (const u32x4*)d_in[0], (const uint16_t*)d_in[4], (const uint16_t*)d_in[5], (char*)d_ws);
}
static inline void launch_main(void* const* d_in, void* d_out, void* d_ws, hipStream_t stream) {
  k_main<<<NB, 512, 0, stream>>>((const char*)d_in[0], (const char*)d_in[1], (const char*)d_ws, (char*)d_out);
}

extern "C" void kernel_launch(void* const* d_in, const int* in_sizes, int n_in, void* d_out, int out_size, void* d_ws, size_t ws_size,
                              hipStream_t stream) {
  launch_part(d_in, d_ws, stream);
  launch_main(d_in, d_out, d_ws, stream);
}
```

```cpp
#include <hip/hip_runtime.h>
#include <stdint.h>

#define LDS __attribute__((address_space(3)))
#define DI __device__ __forceinline__
typedef float f32x16 __attribute__((ext_vector_type(16)));
typedef float f32x2 __attribute__((ext_vector_type(2)));
typedef unsigned u32x4 __attribute__((ext_vector_type(4)));
typedef unsigned u32x2 __attribute__((ext_vector_type(2)));
typedef int i32x4 __attribute__((ext_vector_type(4)));
typedef __bf16 bf16x8 __attribute__((ext_vector_type(8)));
typedef __bf16 bf16x2 __attribute__((ext_vector_type(2)));
typedef short s16x4 __attribute__((ext_vector_type(4)));

constexpr int NN = 50000;
constexpr int NE = 800000;
constexpr int GP = 25;
constexpr int CE = 3200;
constexpr int NC = 250;
constexpr int NB = 256;
constexpr int TROW = 4096;
#define HALF 25000
constexpr int CAP = 512;
constexpr unsigned WS_EP = 0, WS_META = 25600000u, WS_OFF = 28800000u, WS_WF = 29824000u;

#define MFMA(a, b, c) __builtin_amdgcn_mfma_f32_32x32x16_bf16((a), (b), (c), 0, 0, 0)
#define BAR() asm volatile("s_waitcnt lgkmcnt(0)\n\ts_barrier" ::: "memory")
#define SB() __builtin_amdgcn_sched_barrier(0)
#define EPOL "nt"
#define STAMP(k)
#define STAMPC(k, v)

DI unsigned pk(float a, float b) { f32x2 v = {a, b}; bf16x2 r = __builtin_convertvector(v, bf16x2); return __builtin_bit_cast(unsigned, r); }
DI unsigned wave_incl_scan(unsigned x) {
  x += __builtin_amdgcn_update_dpp(0u, x, 0x111, 0xf, 0xf, false);
  x += __builtin_amdgcn_update_dpp(0u, x, 0x112, 0xf, 0xf, false);
  x += __builtin_amdgcn_update_dpp(0u, x, 0x114, 0xf, 0xf, false);
  x += __builtin_amdgcn_update_dpp(0u, x, 0x118, 0xf, 0xf, false);
  x += __builtin_amdgcn_update_dpp(0u, x, 0x142, 0xa, 0xf, false);
  x += __builtin_amdgcn_update_dpp(0u, x, 0x143, 0xc, 0xf, false);
  return x;
}

__global__ __launch_bounds__(512) void k_part(const i32x4* __restrict__ dst4, const i32x4* __restrict__ src4, const u32x4* __restrict__ x16,
                                              const uint16_t* __restrict__ wm, const uint16_t* __restrict__ wu, char* __restrict__ ws) {
  __shared__ __attribute__((aligned(16))) unsigned cnt[2048];
  __shared__ __attribute__((aligned(16))) unsigned metaL[CE];
  __shared__ unsigned wsum[8];
  const int tid = threadIdx.x, c = blockIdx.x;
#undef PROFBUF
#define PROFBUF (ws + (8u << 20))
  if (c >= NC) {
    const int it = (c - NC) * 512 + tid;
    if (it < 34 * 64) {
      const int f = it >> 6, l = it & 63, col = l & 31, h = l >> 5;
      unsigned short v[8];
#pragma unroll
      for (int j = 0; j < 8; ++j) {
        if (f < 18) {
          const int t = f & 1, s = (f >> 1) & 3;
          const int row = (f < 8 ? 0 : (f < 16 ? 64 : 128)) + (f < 16 ? 16 * s : 0) + 8 * h + j;
          v[j] = wm[row * 64 + 32 * t + col];
        } else {
          const int ks = (f - 18) >> 1, u = f & 1;
          const int row = ks < 4 ? 16 * ks + 8 * h + j : 64 + 32 * ((ks - 4) >> 1) + 16 * (ks & 1) + 8 * (j >> 2) + 4 * h + (j & 3);
          v[j] = wu[row * 64 + 32 * u + col];
        }
      }
      u32x4 o = {v[0] | ((unsigned)v[1] << 16), v[2] | ((unsigned)v[3] << 16), v[4] | ((unsigned)v[5] << 16), v[6] | ((unsigned)v[7] << 16)};
      if (f < 18) {
#pragma unroll
        for (int q = 0; q < 4; ++q)
          o[q] = pk(__builtin_bit_cast(float, o[q] << 16) * -1.4426950408889634f, __builtin_bit_cast(float, o[q] & 0xFFFF0000u) * -1.4426950408889634f);
      }
      *(u32x4*)(ws + WS_WF + it * 16) = o;
    }
    return;
  }
  STAMP(0);
  constexpr int TA = CE / 8;
  i32x4 d[2], s[2];
  const int ta = tid < TA ? tid : TA - 1;
#pragma unroll
  for (int i = 0; i < 2; ++i) { d[i] = dst4[c * (CE / 4) + ta + i * TA]; s[i] = src4[c * (CE / 4) + ta + i * TA]; }
  *(u32x4*)&cnt[4 * tid] = u32x4{0, 0, 0, 0};
  BAR();
  STAMP(1);
  unsigned g[8], rk[8];
  if (tid < TA) {
#pragma unroll
    for (int j = 0; j < 8; ++j) { g[j] = ((unsigned)d[j >> 2][j & 3] * 128u + 127u) / 3125u; rk[j] = __hip_atomic_fetch_add(&cnt[g[j]], 1u, __ATOMIC_RELAXED, __HIP_MEMORY_SCOPE_WORKGROUP); }
  }
  BAR();
  STAMP(2);
  const u32x4 cc = *(const u32x4*)&cnt[4 * tid];
  const unsigned tot = cc[0] + cc[1] + cc[2] + cc[3];
  const unsigned incl = wave_incl_scan(tot);
  if ((tid & 63) == 63) wsum[tid >> 6] = incl;
  BAR();
  unsigned ws8 = wsum[tid & 7];
  ws8 += __builtin_amdgcn_update_dpp(0u, ws8, 0x111, 0xf, 0xf, false);
  ws8 += __builtin_amdgcn_update_dpp(0u, ws8, 0x112, 0xf, 0xf, false);
  ws8 += __builtin_amdgcn_update_dpp(0u, ws8, 0x114, 0xf, 0xf, false);
  const int w = __builtin_amdgcn_readfirstlane(tid >> 6);
  const unsigned wb = w ? __builtin_amdgcn_readlane(ws8, w - 1) : 0u;
  const unsigned o0 = wb + incl - tot, o1 = o0 + cc[0], o2 = o1 + cc[1], o3 = o2 + cc[2];
  *(u32x4*)&cnt[4 * tid] = u32x4{o0, o1, o2, o3};
  *(u32x2*)(ws + WS_OFF + c * TROW + tid * 8) = u32x2{o0 | (o1 << 16), o2 | (o3 << 16)};
  BAR();
  STAMP(3);
  if (tid < TA) {
#pragma unroll
    for (int j = 0; j < 8; ++j) {
      const unsigned dv = d[j >> 2][j & 3], sv = s[j >> 2][j & 3];
      metaL[cnt[g[j]] + rk[j]] = sv * (GP * CE) + (dv - (__umul24(g[j], 3125u) >> 7)) * CE + (4 * tid + (j & 3) + (j >> 2) * (4 * TA));
    }
  }
  BAR();
  STAMP(4);
  if (tid < TA) {
#pragma unroll
    for (int i = 0; i < 2; ++i) *(u32x4*)(ws + WS_META + c * (CE * 4) + (tid + i * TA) * 16) = *(const u32x4*)&metaL[4 * (tid + i * TA)];
  }
  STAMP(5);
  STAMP(6);
}

constexpr unsigned L_STG = 0;
constexpr unsigned SLOT = 40960;
constexpr unsigned L_S = 81920;
constexpr unsigned L_LIST = 98304;
constexpr unsigned L_WU = 131072;
constexpr unsigned L_T1 = 147456;
constexpr unsigned L_T2 = 151552;
constexpr unsigned L_PRG = 155648;
constexpr unsigned L_END = 158720;
constexpr unsigned L_M = 0;
constexpr unsigned L_OWN = 40960;
constexpr unsigned L_WIMG = 73728;
constexpr unsigned L_PF = 92160;
#define PFW 1

DI void dma16(unsigned m0, unsigned voff, __amdgpu_buffer_rsrc_t srd, unsigned soff) {
  asm volatile("s_mov_b32 m0, %0\n\ts_nop 0\n\tbuffer_load_dwordx4 %1, %2, %3 offen lds" ::"s"(m0), "v"(voff), "s"(srd), "s"(soff) : "m0", "memory");
}
DI unsigned umax(unsigned a, unsigned b) { return a > b ? a : b; }
DI unsigned wave_excl_max(unsigned x) {
  x = umax(x, __builtin_amdgcn_update_dpp(0u, x, 0x111, 0xf, 0xf, false));
  x = umax(x, __builtin_amdgcn_update_dpp(0u, x, 0x112, 0xf, 0xf, false));
  x = umax(x, __builtin_amdgcn_update_dpp(0u, x, 0x114, 0xf, 0xf, false));
  x = umax(x, __builtin_amdgcn_update_dpp(0u, x, 0x118, 0xf, 0xf, false));
  x = umax(x, __builtin_amdgcn_update_dpp(0u, x, 0x142, 0xa, 0xf, false));
  x = umax(x, __builtin_amdgcn_update_dpp(0u, x, 0x143, 0xc, 0xf, false));
  return __builtin_amdgcn_update_dpp(0u, x, 0x138, 0xf, 0xf, false);
}
DI float silu1(float v) { return v * __builtin_amdgcn_rcpf(1.0f + __builtin_amdgcn_exp2f(v * -1.4426950408889634f)); }

__global__ __launch_bounds__(512, 2) void k_main(const char* __restrict__ x, const char* __restrict__ ea, const char* __restrict__ ws, char* __restrict__ out) {
  __shared__ __attribute__((aligned(1024))) char lds_[L_END];
  LDS char* lds = (LDS char*)lds_;
  const unsigned ldsb = (unsigned)(size_t)lds;
  const int tid = threadIdx.x, l = tid & 63, w = __builtin_amdgcn_readfirstlane(tid >> 6);
  const int xcd = blockIdx.x & 7, b = xcd * 32 + (blockIdx.x >> 3);
  const int g = 8 * b + w, nbase = (g * 3125) >> 7, ncnt = (((g + 1) * 3125) >> 7) - nbase;
  const int r = l & 31, h = l >> 5;
#undef PROFBUF
#define PROFBUF ((char*)ws)
  STAMP(0);
  const auto sx = __builtin_amdgcn_make_buffer_rsrc((void*)x, 0, NN * 128, 0x00020000);
  const auto se = __builtin_amdgcn_make_buffer_rsrc((void*)ea, 0, NE * 32, 0x00020000);
  const auto sm = __builtin_amdgcn_make_buffer_rsrc((void*)(ws + WS_META), 0, NE * 4, 0x00020000);
  const auto sw = __builtin_amdgcn_make_buffer_rsrc((void*)(ws + WS_WF), 0, 34 * 1024, 0x00020000);

  const auto st = __builtin_amdgcn_make_buffer_rsrc((void*)(ws + WS_OFF), 0, NC * TROW, 0x00020000);

  const unsigned stg = L_STG + w * 5120;
  asm volatile("s_nop 4" ::: "memory");
  dma16(ldsb + L_T1 + w * 1024, (tid & 255) * TROW + b * 16 + (w >> 2) * 16, st, 0);
  const unsigned bx = blockIdx.x;
  {
    unsigned long long pc; unsigned len;
    asm volatile("s_getpc_b64 %0\n.Lkm_code_here:\n\ts_mov_b32 %1, .Lkm_code_end-.Lkm_code_here" : "=s"(pc), "=s"(len));
    const auto sc = __builtin_amdgcn_make_buffer_rsrc((void*)pc, 0, len, 0x00020000);
    const unsigned vo = l < 16 ? (w * 16 + l) * 128 : 0x7FFFFFF0u;
    asm volatile("s_cmp_lt_u32 %3, %4\n\ts_cbranch_scc0 1f\n\ts_nop 4\n\ts_mov_b32 m0, %0\n\ts_nop 0\n\tbuffer_load_dword %1, %2, 0 offen lds\n\t1:"
                 ::"s"(ldsb + L_PF + w * 256), "v"(vo), "s"(sc), "s"(bx), "n"(8 * PFW) : "m0", "scc", "memory");
  }
  const unsigned sreg = L_S + w * 2048;
  const unsigned la = L_LIST + w * 4096, lb = la + 2048;
  *(LDS u32x4*)(lds + lb + l * 16) = u32x4{512, 512, 512, 512};
  *(LDS u32x4*)(lds + lb + 1024 + l * 16) = u32x4{512, 512, 512, 512};
  f32x16 Y0, Y1;
#pragma unroll
  for (int i = 0; i < 16; ++i) { Y0[i] = 0.f; Y1[i] = 0.f; }

  unsigned xch[4];
#pragma unroll
  for (int s = 0; s < 4; ++s) xch[s] = ((l & 7) ^ (((8 * s + (l >> 3)) >> 1) & 7)) << 4;
  unsigned ech = ((l & 1) ^ ((l >> 4) & 1)) << 4;
  unsigned skey = sreg + (l >> 1) * 64 + (((l >> 3) & 3) << 4);
  unsigned aoff = la + (l >> 3) * 16;
  unsigned boff = lb + (4 * ((l >> 1) & 7) + (l >> 4)) * 4;
  unsigned xrd[4];
#pragma unroll
  for (int s = 0; s < 4; ++s) xrd[s] = stg + r * 128 + (((2 * s + h) ^ ((r >> 1) & 7)) << 4);
  unsigned erd = stg + 4096 + r * 32 + ((h ^ ((r >> 3) & 1)) << 4);
  unsigned srow0 = sreg + r * 64 + ((h ^ ((r >> 2) & 3)) << 4), srow1 = srow0 ^ 32;
  unsigned str0 = sreg + (4 * h + ((l & 15) >> 2)) * 64 + (((2 * ((l >> 4) & 1) + ((l & 3) >> 1)) ^ h) << 4) + (l & 1) * 8;
  unsigned str1 = (str0 ^ 32) + 512;

  asm volatile("" : "+v"(xch[0]), "+v"(xch[1]), "+v"(ech), "+v"(skey), "+v"(aoff), "+v"(boff), "+v"(xrd[0]), "+v"(xrd[1]), "+v"(xrd[2]), "+v"(xrd[3]));
  asm volatile("" : "+v"(erd), "+v"(srow0), "+v"(srow1), "+v"(str0), "+v"(str1), "+v"(Y0), "+v"(Y1));
  xch[2] = xch[0]; xch[3] = xch[1];
  asm volatile("s_cmp_lt_u32 %0, %1\n\ts_cbranch_scc0 1f\n\ts_waitcnt vmcnt(1)\n\ts_branch 2f\n\t1:\n\ts_waitcnt vmcnt(0)\n\t2:" ::"s"(bx), "n"(8 * PFW) : "scc", "memory");
  if (b == NB - 1 && w >= 4) *(LDS unsigned short*)(lds + L_T2 + (tid & 255) * 16) = (tid & 255) < NC ? CE : 0;
  BAR();
  STAMP(1);
  {
    const unsigned c0 = 32 * w + (l >> 3);
    unsigned s0[4], s1[4], vo[4];
#pragma unroll
    for (int i = 0; i < 4; ++i) { s0[i] = *(LDS unsigned short*)(lds + L_T1 + c0 * 16 + i * 128); s1[i] = *(LDS unsigned short*)(lds + L_T2 + c0 * 16 + i * 128); }
#pragma unroll
    for (int i = 0; i < 4; ++i) {
      const unsigned o = (s0[i] & ~3u) + 4 * (l & 7);
      vo[i] = o < s1[i] ? (__umul24(c0 + 8 * i, CE) + o) * 4 : 0x7FFFFFF0u;
    }
#pragma unroll
    for (int i = 0; i < 4; ++i) dma16(ldsb + L_M + (32 * w + 8 * i) * 128, vo[i], sm, 0);
  }

  int wb = 0, n, nw;
  unsigned cj[8], mt[8];
  auto window_scan = [&]() {
    int l = tid & 63;
    asm volatile("" : "+v"(l));
    unsigned s0[4], o0[4], o1[4], cn[4], rb[4], ix[4];
    const unsigned ta = L_T1 + l * 16, tb = ta + w * 2, tc = l * 16 + (w < 7 ? L_T1 + w * 2 + 2 : L_T2);
#pragma unroll
    for (int k = 0; k < 4; ++k) {
      s0[k] = *(LDS unsigned short*)(lds + ta + k * 1024);
      o0[k] = *(LDS unsigned short*)(lds + tb + k * 1024);
      o1[k] = *(LDS unsigned short*)(lds + tc + k * 1024);
    }
    unsigned tot = 0;
#pragma unroll
    for (int k = 0; k < 4; ++k) {
      cn[k] = o1[k] - o0[k];
      if (k == 3) cn[k] = l < NC - 192 ? cn[k] : 0u;
      rb[k] = (l << 13) + o0[k] - (s0[k] & ~3u) + ((k << 19) + 512 + (1 << 21));
      ix[k] = tot;
      tot += cn[k];
    }
    const unsigned incl = wave_incl_scan(tot);
    n = __builtin_amdgcn_readlane(incl, 63);
    nw = n - wb < CAP ? n - wb : CAP;
    const int base = (int)(incl - tot) - wb;
#pragma unroll
    for (int k = 0; k < 4; ++k) {
      const int lo = base + (int)ix[k];
      const int pos = lo > 0 ? lo : 0;
      const bool on = pos < lo + (int)cn[k] && pos < CAP;
      *(LDS unsigned*)(lds + (on ? lb + pos * 4 : la + l * 4)) = (pos << 21) + (rb[k] - lo);
    }
    asm volatile("" ::: "memory");
    const u32x4 m0 = *(LDS u32x4*)(lds + lb + l * 32), m1 = *(LDS u32x4*)(lds + lb + l * 32 + 16);
    unsigned run[8];
    run[0] = m0[0];
#pragma unroll
    for (int j = 1; j < 8; ++j) run[j] = umax(run[j - 1], j < 4 ? m0[j] : m1[j - 4]);
    const unsigned ex = wave_excl_max(run[7]);
    const int last = (nw > 0 ? nw - 1 : 0) - 512, i0 = 8 * l - 512;
#pragma unroll
    for (int j = 0; j < 8; ++j) {
      const int im = i0 < last - j ? i0 : last - j;
      cj[j] = (umax(run[j], ex) & 0x1FFFFFu) + im + j;
    }
  };
  auto window_gather = [&](bool all) {
#pragma unroll
    for (int j = 0; j < 8; ++j) {
      const unsigned c = cj[j] >> 13, q = cj[j] & 0x1FFFu;
      const unsigned s0 = *(LDS unsigned short*)(lds + L_T1 + c * 16) & ~3u;
      const unsigned v = __builtin_amdgcn_raw_buffer_load_b32(sm, (__umul24(c, CE) + s0 + q) * 4, 0, 0);
      mt[j] = (all || q >= 32) ? v : mt[j];
    }
  };
  window_scan();
  unsigned pa = L_PRG + w * 256 + l * 4;
  asm volatile("" : "+v"(pa));
  *(LDS int*)(lds + pa) = n; *(LDS int*)(lds + pa + 2048) = n;
#pragma unroll
  for (int i = 0; i < 5; ++i) {
    const int f = w + 8 * i;
    const unsigned lo = (i < 4 ? L_WIMG : L_WU - 18 * 1024) + f * 1024, hi = i < 4 ? L_WU - 18 * 1024 + f * 1024 : L_PF + 2048;
    dma16(ldsb + (i < 2 ? lo : (i == 3 ? hi : (w < 2 ? lo : hi))), l * 16, sw, (i < 4 ? f : (w < 2 ? f : 33)) * 1024);
  }
  const unsigned own = L_OWN + w * 4096;
#pragma unroll
  for (int s = 0; s < 4; ++s) {
    const int R = 8 * s + (l >> 3);
    int node = nbase + R; node = node < NN ? node : NN - 1;
    dma16(ldsb + own + s * 1024, node * 128 + (((l & 7) ^ ((R >> 1) & 7)) << 4), sx, 0);
  }

  STAMP(2);
  asm volatile("s_waitcnt vmcnt(9)" ::: "memory");
  BAR();
  {
    unsigned acc = 0;
#pragma unroll
    for (int j = 0; j < 8; ++j) {
      acc |= cj[j];
      mt[j] = *(LDS unsigned*)(lds + L_M + (((cj[j] >> 13) << 5) + (cj[j] & 31)) * 4);
    }
    const bool over = (acc & 0x1FE0u) != 0;
    BAR();
    if (__any(over)) window_gather(false);
  }
  int nt;
  u32x4 A4;
  unsigned Bq[2];
  auto dma_tile = [&](const int slot, const int tt, const u32x4 A, const unsigned B) {
    const unsigned v0 = A[0] | xch[0], v1 = A[1] | xch[1], v2 = A[2] | xch[2], v3 = A[3] | xch[3], v4 = (B & 0x07FFFFFFu) | ech;
    asm volatile("s_waitcnt lgkmcnt(0)\n\t"
                 "s_cmp_lt_i32 %[t], %[nt]\n\ts_cbranch_scc0 1f\n\t"
                 "s_mov_b32 m0, %[m]\n\ts_nop 0\n\tbuffer_load_dwordx4 %[v0], %[sx], 0 offen lds\n\t"
                 "s_add_u32 m0, %[m], 0x400\n\ts_nop 0\n\tbuffer_load_dwordx4 %[v1], %[sx], 0 offen lds\n\t"
                 "s_add_u32 m0, %[m], 0x800\n\ts_nop 0\n\tbuffer_load_dwordx4 %[v2], %[sx], 0 offen lds\n\t"
                 "s_add_u32 m0, %[m], 0xc00\n\ts_nop 0\n\tbuffer_load_dwordx4 %[v3], %[sx], 0 offen lds\n\t"
                 "s_add_u32 m0, %[m], 0x1000\n\ts_nop 0\n\tbuffer_load_dwordx4 %[v4], %[se], 0 offen " EPOL " lds\n\t"
                 "1:"
                 ::[t] "s"(tt), [nt] "s"(nt), [m] "s"(ldsb + stg + slot * SLOT), [v0] "v"(v0), [v1] "v"(v1), [v2] "v"(v2), [v3] "v"(v3), [v4] "v"(v4), [sx] "s"(sx), [se] "s"(se)
                 : "m0", "scc", "memory");
  };
  auto wait_dma = [&](const int tt) {
    asm volatile("s_cmp_lt_i32 %0, %1\n\ts_cbranch_scc1 1f\n\ts_waitcnt vmcnt(0)\n\t1:\n\ts_waitcnt vmcnt(5)" ::"s"(tt), "s"(nt) : "scc", "memory");
  };
  auto window_lists = [&]() {
    nt = (nw + 31) >> 5;
    {
      int l = tid & 63;
      asm volatile("" : "+v"(l));
      unsigned av[8], bv[8];
#pragma unroll
      for (int j = 0; j < 8; ++j) {
        const unsigned t = mt[j] >> 7, sn = __umulhi(t, 54975582u) >> 3;
        const unsigned u = (unsigned)__mul24((int)sn, -625) + t;
        const unsigned dl = __umul24(u, 41u) >> 10;
        const unsigned eh = (unsigned)__mul24((int)dl, -25) + __umul24(cj[j] >> 13, 25u) + u;
        av[j] = sn << 7;
        bv[j] = (eh << 12) | ((mt[j] << 5) & 0xFE0u) | ((8 * l + j < nw ? dl : 31u) << 27);
      }
      unsigned cl = 0;
      bool lo[8];
#pragma unroll
      for (int j = 0; j < 8; ++j) { lo[j] = av[j] < HALF * 128u && 8 * l + j < nw; cl += lo[j]; }
      const unsigned incl = wave_incl_scan(cl);
      unsigned pl = incl - cl;
      const unsigned hb = (unsigned)__builtin_amdgcn_readlane(incl, 63) + 8 * l;
#pragma unroll
      for (int j = 0; j < 8; ++j) {
        const unsigned pos = lo[j] ? pl : hb + j - pl;
        pl += lo[j];
        *(LDS unsigned*)(lds + la + pos * 4) = av[j];
        *(LDS unsigned*)(lds + la + pos * 4 + 2048) = bv[j];
      }
      asm volatile("" ::: "memory");
    }
    A4 = *(LDS u32x4*)(lds + aoff); Bq[0] = *(LDS unsigned*)(lds + boff);
    dma_tile(0, 0, A4, Bq[0]);
  };
  window_lists();
  wait_dma(0);
  BAR();
  STAMP(3);

  bf16x8 W1[4][2], W3[2], P2[2][2], XO[4];
  {
    auto wfrag = [&](int f) { return *(LDS bf16x8*)(lds + L_WIMG + f * 1024 + l * 16); };
#pragma unroll
    for (int s = 0; s < 4; ++s) { W1[s][0] = wfrag(2 * s); W1[s][1] = wfrag(2 * s + 1); }
    W3[0] = wfrag(16); W3[1] = wfrag(17);
    const int rs = (r & 19) | ((r & 4) << 1) | ((r & 8) >> 1);
    f32x16 p0, p1;
#pragma unroll
    for (int i = 0; i < 16; ++i) { p0[i] = 0.f; p1[i] = 0.f; }
#pragma unroll
    for (int s = 0; s < 4; ++s) {
      const bf16x8 a = *(LDS bf16x8*)(lds + own + rs * 128 + (((2 * s + h) ^ ((rs >> 1) & 7)) << 4));
      p0 = MFMA(a, wfrag(8 + 2 * s), p0);
      p1 = MFMA(a, wfrag(9 + 2 * s), p1);
      XO[s] = *(LDS bf16x8*)(lds + own + r * 128 + (((2 * s + h) ^ ((r >> 1) & 7)) << 4));
    }
#pragma unroll
    for (int s = 0; s < 2; ++s) {
      P2[s][0] = __builtin_bit_cast(bf16x8, u32x4{pk(p0[8 * s], p0[8 * s + 1]), pk(p0[8 * s + 2], p0[8 * s + 3]), pk(p0[8 * s + 4], p0[8 * s + 5]), pk(p0[8 * s + 6], p0[8 * s + 7])});
      P2[s][1] = __builtin_bit_cast(bf16x8, u32x4{pk(p1[8 * s], p1[8 * s + 1]), pk(p1[8 * s + 2], p1[8 * s + 3]), pk(p1[8 * s + 4], p1[8 * s + 5]), pk(p1[8 * s + 6], p1[8 * s + 7])});
    }
  }
  BAR();
  *(LDS u32x4*)(lds + sreg + l * 16) = u32x4{0, 0, 0, 0};
  *(LDS u32x4*)(lds + sreg + 1024 + l * 16) = u32x4{0, 0, 0, 0};
  while (true) {
    STAMP(4); STAMPC(0, __builtin_amdgcn_s_memtime()); STAMPC(2, nt);
    f32x16 a0, a1;
    bf16x8 xf[4], ef, sr0, sr1;
    s16x4 q00 = {0, 0, 0, 0}, q01 = q00, q10 = q00, q11 = q00;
    u32x4 mm0 = {0, 0, 0, 0}, mm1 = mm0;
    unsigned sa;
    int oth, mine;
    f32x16 zero;
#pragma unroll
    for (int i = 0; i < 16; ++i) zero[i] = 0.f;
    auto rd_frags = [&](const int slot) {
#pragma unroll
      for (int s = 0; s < 4; ++s) xf[s] = *(LDS bf16x8*)(lds + xrd[s] + slot * SLOT);
      ef = *(LDS bf16x8*)(lds + erd + slot * SLOT);
      sr0 = *(LDS bf16x8*)(lds + srow0); sr1 = *(LDS bf16x8*)(lds + srow1);
    };
    asm volatile("" ::: "memory");
    {
      const u32x4 A1 = *(LDS u32x4*)(lds + aoff + 128); Bq[1] = *(LDS unsigned*)(lds + boff + 128);
      dma_tile(1, 1, A1, Bq[1]);
      wait_dma(1);
      sa = (Bq[0] >> 26) ^ skey;
      *(LDS unsigned short*)(lds + sa) = 0x3F80;
      asm volatile("" ::: "memory");
      rd_frags(0);
      A4 = *(LDS u32x4*)(lds + aoff + 256); Bq[0] = *(LDS unsigned*)(lds + boff + 256);
      dma_tile(0, 2, A4, Bq[0]);
      a0 = MFMA(sr0, P2[0][0], zero); a0 = MFMA(sr1, P2[1][0], a0);
#pragma unroll
      for (int s = 0; s < 4; ++s) a0 = MFMA(xf[s], W1[s][0], a0);
      a0 = MFMA(ef, W3[0], a0);
    }
#define S0_ __builtin_bit_cast(bf16x8, __builtin_shufflevector(q00, q01, 0, 1, 2, 3, 4, 5, 6, 7))
#define S1_ __builtin_bit_cast(bf16x8, __builtin_shufflevector(q10, q11, 0, 1, 2, 3, 4, 5, 6, 7))
#define BF(v) __builtin_bit_cast(bf16x8, v)
#define PIN(lo, hi) _Pragma("unroll") for (int i = lo; i < hi; i += 4) asm volatile("" : "+v"(u[i]), "+v"(u[i + 1]), "+v"(u[i + 2]), "+v"(u[i + 3])); SB();
#define EXPS(lo, hi) _Pragma("unroll") for (int i = lo; i < hi; ++i) u[i] = __builtin_amdgcn_exp2f(c[i]); PIN(lo, hi)
#define ADDS(lo, hi) _Pragma("unroll") for (int i = lo; i < hi; ++i) u[i] = 1.0f + u[i]; PIN(lo, hi)
#define RCPS(lo, hi) _Pragma("unroll") for (int i = lo; i < hi; ++i) u[i] = __builtin_amdgcn_rcpf(u[i]); PIN(lo, hi)
#define FINS(lo, hi) _Pragma("unroll") for (int i = lo; i < hi; ++i) u[i] *= c[i]; PIN(lo, hi)
    auto phase = [&](const int T, const int par, const int t, f32x16& c, f32x16& nx, f32x16& Yc, f32x16& Yp) {
      float u[16];
      SB();
      Yp = MFMA(BF(mm1), S1_, Yp); SB();
      if (T == 0) {
        q00 = __builtin_amdgcn_ds_read_tr16_b64_v4i16((LDS s16x4*)(lds + str0));
        q01 = __builtin_amdgcn_ds_read_tr16_b64_v4i16((LDS s16x4*)(lds + str1));
        q10 = __builtin_amdgcn_ds_read_tr16_b64_v4i16((LDS s16x4*)(lds + str0 + 1024));
        q11 = __builtin_amdgcn_ds_read_tr16_b64_v4i16((LDS s16x4*)(lds + str1 + 1024));
        asm volatile("" ::: "memory");
        *(LDS unsigned short*)(lds + sa) = 0;
        sa = (Bq[par ^ 1] >> 26) ^ skey;
        SB();
      } else {
        dma_tile(par ^ 1, t + 3, A4, Bq[par ^ 1]);
        SB();
      }
      const int U = T ^ 1;
      nx = MFMA(sr0, P2[0][U], zero); SB();
      EXPS(0, 4)
      nx = MFMA(sr1, P2[1][U], nx); SB();
      EXPS(4, 8)
      nx = MFMA(xf[0], W1[0][U], nx); SB();
      EXPS(8, 12)
      nx = MFMA(xf[1], W1[1][U], nx); SB();
      EXPS(12, 16)
      nx = MFMA(xf[2], W1[2][U], nx); SB();
      ADDS(0, 16)
      nx = MFMA(xf[3], W1[3][U], nx); SB();
      RCPS(0, 4)
      nx = MFMA(ef, W3[U], nx); SB();
      if (T == 0) {
        wait_dma(t + 2);
        *(LDS unsigned short*)(lds + sa) = 0x3F80;
        asm volatile("" ::: "memory");
        rd_frags(par ^ 1);
        A4 = *(LDS u32x4*)(lds + aoff + (t + 3) * 128);
        Bq[par ^ 1] = *(LDS unsigned*)(lds + boff + (t + 3) * 128);
        const int left = n - wb - 32 * t;
        *(LDS int*)(lds + pa) = left; *(LDS int*)(lds + pa + 2048) = left;
        oth = *(LDS int*)(lds + pa + 1024);
        SB();
      }
      if (T == 1) {
        const int o = __builtin_amdgcn_readfirstlane(oth), left = __builtin_amdgcn_readfirstlane(n - wb - 32 * t);
        asm volatile("s_cmp_ge_i32 %0, %1\n\ts_cbranch_scc1 1f\n\ts_setprio 0\n\ts_branch 2f\n\t1:\n\ts_setprio 1\n\t2:" ::"s"(left), "s"(o) : "scc");
        SB();
      }
      RCPS(4, 16)
      FINS(0, 16)
      mm0 = u32x4{pk(u[0], u[1]), pk(u[2], u[3]), pk(u[4], u[5]), pk(u[6], u[7])}; SB();
      mm1 = u32x4{pk(u[8], u[9]), pk(u[10], u[11]), pk(u[12], u[13]), pk(u[14], u[15])}; SB();
      Yc = MFMA(BF(mm0), S0_, Yc); SB();
    };
    for (int t = 0; t < nt; t += 2) {
      phase(0, 0, t, a0, a1, Y0, Y1);
      phase(1, 0, t, a1, a0, Y1, Y0);
      if (t + 1 >= nt) break;
      phase(0, 1, t + 1, a0, a1, Y0, Y1);
      phase(1, 1, t + 1, a1, a0, Y1, Y0);
    }
    Y1 = MFMA(BF(mm1), S1_, Y1);
    *(LDS unsigned short*)(lds + sa) = 0;
    wb += CAP;
    if (wb >= n) break;
    *(LDS u32x4*)(lds + lb + (tid & 63) * 16) = u32x4{512, 512, 512, 512};
    *(LDS u32x4*)(lds + lb + 1024 + (tid & 63) * 16) = u32x4{512, 512, 512, 512};
    asm volatile("" ::: "memory");
    window_scan();
    window_gather(true);
    window_lists();
  }

  STAMP(5); STAMPC(1, __builtin_amdgcn_s_memtime());
#pragma unroll
  for (int i = 0; i < 16; ++i) { Y0[i] *= -0.6931471805599453f; Y1[i] *= -0.6931471805599453f; }
  f32x16 o0, o1;
#pragma unroll
  for (int i = 0; i < 16; ++i) { o0[i] = 0.f; o1[i] = 0.f; }
  auto ufrag = [&](int f) { return *(LDS bf16x8*)(lds + L_WU + f * 1024 + l * 16); };
#pragma unroll
  for (int s = 0; s < 4; ++s) { o0 = MFMA(ufrag(2 * s), XO[s], o0); o1 = MFMA(ufrag(2 * s + 1), XO[s], o1); }
#pragma unroll
  for (int s = 0; s < 2; ++s) {
    const bf16x8 z0 = __builtin_bit_cast(bf16x8, u32x4{pk(Y0[8 * s], Y0[8 * s + 1]), pk(Y0[8 * s + 2], Y0[8 * s + 3]), pk(Y0[8 * s + 4], Y0[8 * s + 5]), pk(Y0[8 * s + 6], Y0[8 * s + 7])});
    const bf16x8 z1 = __builtin_bit_cast(bf16x8, u32x4{pk(Y1[8 * s], Y1[8 * s + 1]), pk(Y1[8 * s + 2], Y1[8 * s + 3]), pk(Y1[8 * s + 4], Y1[8 * s + 5]), pk(Y1[8 * s + 6], Y1[8 * s + 7])});
    o0 = MFMA(ufrag(8 + 2 * s), z0, o0); o1 = MFMA(ufrag(9 + 2 * s), z0, o1);
    o0 = MFMA(ufrag(12 + 2 * s), z1, o0); o1 = MFMA(ufrag(13 + 2 * s), z1, o1);
  }
#pragma unroll
  for (int q = 0; q < 4; ++q) {
    const u32x2 v0 = {pk(silu1(o0[4 * q]), silu1(o0[4 * q + 1])), pk(silu1(o0[4 * q + 2]), silu1(o0[4 * q + 3]))};
    const u32x2 v1 = {pk(silu1(o1[4 * q]), silu1(o1[4 * q + 1])), pk(silu1(o1[4 * q + 2]), silu1(o1[4 * q + 3]))};
    *(LDS u32x2*)(lds + stg + r * 128 + ((q ^ (r & 7)) << 4) + h * 8) = v0;
    *(LDS u32x2*)(lds + stg + r * 128 + (((4 + q) ^ (r & 7)) << 4) + h * 8) = v1;
  }
  asm volatile("" ::: "memory");
#pragma unroll
  for (int it = 0; it < 4; ++it) {
    const int m = l + 64 * it, row = m >> 3, p = m & 7;
    if (m < ncnt * 8) {
      const u32x4 v = *(LDS u32x4*)(lds + stg + row * 128 + ((p ^ (row & 7)) << 4));
      *(u32x4*)(out + (size_t)nbase * 128 + m * 16) = v;
    }
  }
  STAMP(6);
  asm volatile(".Lkm_code_end:");
}

static inline void launch_part(void* const* d_in, void* d_ws, hipStream_t stream) {
  k_part<<<NC + 5, 512, 0, stream>>>((const i32x4*)d_in[3], (const i32x4*)d_in[2], (const u32x4*)d_in[0], (const uint16_t*)d_in[4], (const uint16_t*)d_in[5], (char*)d_ws);
}
static inline void launch_main(void* const* d_in, void* d_out, void* d_ws, hipStream_t stream) {
  k_main<<<NB, 512, 0, stream>>>((const char*)d_in[0], (const char*)d_in[1], (const char*)d_ws, (char*)d_out);
}

extern "C" void kernel_launch(void* const* d_in, const int* in_sizes, int n_in, void* d_out, int out_size, void* d_ws, size_t ws_size,
                              hipStream_t stream) {
  launch_part(d_in, d_ws, stream);
  launch_main(d_in, d_out, d_ws, stream);
}
```

```cpp
#include <hip/hip_runtime.h>
#include <stdint.h>

#define LDS __attribute__((address_space(3)))
#define DI __device__ __forceinline__
typedef float f32x16 __attribute__((ext_vector_type(16)));
typedef float f32x2 __attribute__((ext_vector_type(2)));
typedef unsigned u32x4 __attribute__((ext_vector_type(4)));
typedef unsigned u32x2 __attribute__((ext_vector_type(2)));
typedef int i32x4 __attribute__((ext_vector_type(4)));
typedef __bf16 bf16x8 __attribute__((ext_vector_type(8)));
typedef __bf16 bf16x2 __attribute__((ext_vector_type(2)));
typedef short s16x4 __attribute__((ext_vector_type(4)));

constexpr int NN = 50000;
constexpr int NE = 800000;
constexpr int GP = 25;
constexpr int CE = 3200;
constexpr int NC = 250;
constexpr int NB = 256;
constexpr int TROW = 4096;
#define HALF 25000
constexpr int CAP = 512;
constexpr unsigned WS_EP = 0, WS_META = 25600000u, WS_OFF = 28800000u, WS_WF = 29824000u;

#define MFMA(a, b, c) __builtin_amdgcn_mfma_f32_32x32x16_bf16((a), (b), (c), 0, 0, 0)
#define BAR() asm volatile("s_waitcnt lgkmcnt(0)\n\ts_barrier" ::: "memory")
#define SB() __builtin_amdgcn_sched_barrier(0)
#define EPOL "nt"
#define STAMP(k)
#define STAMPC(k, v)

DI unsigned pk(float a, float b) { f32x2 v = {a, b}; bf16x2 r = __builtin_convertvector(v, bf16x2); return __builtin_bit_cast(unsigned, r); }
DI unsigned wave_incl_scan(unsigned x) {
  x += __builtin_amdgcn_update_dpp(0u, x, 0x111, 0xf, 0xf, false);
  x += __builtin_amdgcn_update_dpp(0u, x, 0x112, 0xf, 0xf, false);
  x += __builtin_amdgcn_update_dpp(0u, x, 0x114, 0xf, 0xf, false);
  x += __builtin_amdgcn_update_dpp(0u, x, 0x118, 0xf, 0xf, false);
  x += __builtin_amdgcn_update_dpp(0u, x, 0x142, 0xa, 0xf, false);
  x += __builtin_amdgcn_update_dpp(0u, x, 0x143, 0xc, 0xf, false);
  return x;
}

__global__ __launch_bounds__(512) void k_part(const i32x4* __restrict__ dst4, const i32x4* __restrict__ src4, const u32x4* __restrict__ x16,
                                              const uint16_t* __restrict__ wm, const uint16_t* __restrict__ wu, char* __restrict__ ws) {
  __shared__ __attribute__((aligned(16))) unsigned cnt[2048];
  __shared__ __attribute__((aligned(16))) unsigned metaL[CE];
  __shared__ unsigned wsum[8];
  const int tid = threadIdx.x, c = blockIdx.x;
#undef PROFBUF
#define PROFBUF (ws + (8u << 20))
  if (c >= NC) {
    const int it = (c - NC) * 512 + tid;
    if (it < 34 * 64) {
      const int f = it >> 6, l = it & 63, col = l & 31, h = l >> 5;
      unsigned short v[8];
#pragma unroll
      for (int j = 0; j < 8; ++j) {
        if (f < 18) {
          const int t = f & 1, s = (f >> 1) & 3;
          const int row = (f < 8 ? 0 : (f < 16 ? 64 : 128)) + (f < 16 ? 16 * s : 0) + 8 * h + j;
          v[j] = wm[row * 64 + 32 * t + col];
        } else {
          const int ks = (f - 18) >> 1, u = f & 1;
          const int row = ks < 4 ? 16 * ks + 8 * h + j : 64 + 32 * ((ks - 4) >> 1) + 16 * (ks & 1) + 8 * (j >> 2) + 4 * h + (j & 3);
          v[j] = wu[row * 64 + 32 * u + col];
        }
      }
      u32x4 o = {v[0] | ((unsigned)v[1] << 16), v[2] | ((unsigned)v[3] << 16), v[4] | ((unsigned)v[5] << 16), v[6] | ((unsigned)v[7] << 16)};
      if (f < 18) {
#pragma unroll
        for (int q = 0; q < 4; ++q)
          o[q] = pk(__builtin_bit_cast(float, o[q] << 16) * -1.4426950408889634f, __builtin_bit_cast(float, o[q] & 0xFFFF0000u) * -1.4426950408889634f);
      }
      *(u32x4*)(ws + WS_WF + it * 16) = o;
    }
    return;
  }
  STAMP(0);
  constexpr int TA = CE / 8;
  i32x4 d[2], s[2];
  const int ta = tid < TA ? tid : TA - 1;
#pragma unroll
  for (int i = 0; i < 2; ++i) { d[i] = dst4[c * (CE / 4) + ta + i * TA]; s[i] = src4[c * (CE / 4) + ta + i * TA]; }
  unsigned cw[2] = {0, 0};
  if (c < 8 && __builtin_amdgcn_readfirstlane(tid >> 6) == 7) {
    unsigned long long pc; int off; unsigned len;
    asm volatile("s_getpc_b64 %0\n.Lkp_code_here:\n\ts_mov_b32 %1, .Lkm_code_begin-.Lkp_code_here\n\ts_mov_b32 %2, .Lkm_code_end-.Lkm_code_begin" : "=s"(pc), "=s"(off), "=s"(len));
    typedef __attribute__((address_space(1))) const unsigned* gptr;
    const unsigned long long base = (pc + off) & ~255ull;
#pragma unroll
    for (int i = 0; i < 2; ++i) { const unsigned o = (i * 64 + (tid & 63)) * 128; cw[i] = *(gptr)(base + (o < len - 4 ? o : len - 4)); }
  }
  *(u32x4*)&cnt[4 * tid] = u32x4{0, 0, 0, 0};
  BAR();
  STAMP(1);
  unsigned g[8], rk[8];
  if (tid < TA) {
#pragma unroll
    for (int j = 0; j < 8; ++j) { g[j] = ((unsigned)d[j >> 2][j & 3] * 128u + 127u) / 3125u; rk[j] = __hip_atomic_fetch_add(&cnt[g[j]], 1u, __ATOMIC_RELAXED, __HIP_MEMORY_SCOPE_WORKGROUP); }
  }
  BAR();
  STAMP(2);
  const u32x4 cc = *(const u32x4*)&cnt[4 * tid];
  const unsigned tot = cc[0] + cc[1] + cc[2] + cc[3];
  const unsigned incl = wave_incl_scan(tot);
  if ((tid & 63) == 63) wsum[tid >> 6] = incl;
  BAR();
  unsigned ws8 = wsum[tid & 7];
  ws8 += __builtin_amdgcn_update_dpp(0u, ws8, 0x111, 0xf, 0xf, false);
  ws8 += __builtin_amdgcn_update_dpp(0u, ws8, 0x112, 0xf, 0xf, false);
  ws8 += __builtin_amdgcn_update_dpp(0u, ws8, 0x114, 0xf, 0xf, false);
  const int w = __builtin_amdgcn_readfirstlane(tid >> 6);
  const unsigned wb = w ? __builtin_amdgcn_readlane(ws8, w - 1) : 0u;
  const unsigned o0 = wb + incl - tot, o1 = o0 + cc[0], o2 = o1 + cc[1], o3 = o2 + cc[2];
  *(u32x4*)&cnt[4 * tid] = u32x4{o0, o1, o2, o3};
  *(u32x2*)(ws + WS_OFF + c * TROW + tid * 8) = u32x2{o0 | (o1 << 16), o2 | (o3 << 16)};
  BAR();
  STAMP(3);
  if (tid < TA) {
#pragma unroll
    for (int j = 0; j < 8; ++j) {
      const unsigned dv = d[j >> 2][j & 3], sv = s[j >> 2][j & 3];
      metaL[cnt[g[j]] + rk[j]] = sv * (GP * CE) + (dv - (__umul24(g[j], 3125u) >> 7)) * CE + (4 * tid + (j & 3) + (j >> 2) * (4 * TA));
    }
  }
  BAR();
  STAMP(4);
  if (tid < TA) {
#pragma unroll
    for (int i = 0; i < 2; ++i) *(u32x4*)(ws + WS_META + c * (CE * 4) + (tid + i * TA) * 16) = *(const u32x4*)&metaL[4 * (tid + i * TA)];
  }
  asm volatile("" ::"v"(cw[0]), "v"(cw[1]));
  STAMP(5);
  STAMP(6);
}

constexpr unsigned L_STG = 0;
constexpr unsigned SLOT = 40960;
constexpr unsigned L_S = 81920;
constexpr unsigned L_LIST = 98304;
constexpr unsigned L_WU = 131072;
constexpr unsigned L_T1 = 147456;
constexpr unsigned L_T2 = 151552;
constexpr unsigned L_PRG = 155648;
constexpr unsigned L_END = 158720;
constexpr unsigned L_M = 0;
constexpr unsigned L_OWN = 40960;
constexpr unsigned L_WIMG = 73728;
constexpr unsigned L_PF = 92160;
#define PFW 1

DI void dma16(unsigned m0, unsigned voff, __amdgpu_buffer_rsrc_t srd, unsigned soff) {
  asm volatile("s_mov_b32 m0, %0\n\ts_nop 0\n\tbuffer_load_dwordx4 %1, %2, %3 offen lds" ::"s"(m0), "v"(voff), "s"(srd), "s"(soff) : "m0", "memory");
}
DI unsigned umax(unsigned a, unsigned b) { return a > b ? a : b; }
DI unsigned wave_excl_max(unsigned x) {
  x = umax(x, __builtin_amdgcn_update_dpp(0u, x, 0x111, 0xf, 0xf, false));
  x = umax(x, __builtin_amdgcn_update_dpp(0u, x, 0x112, 0xf, 0xf, false));
  x = umax(x, __builtin_amdgcn_update_dpp(0u, x, 0x114, 0xf, 0xf, false));
  x = umax(x, __builtin_amdgcn_update_dpp(0u, x, 0x118, 0xf, 0xf, false));
  x = umax(x, __builtin_amdgcn_update_dpp(0u, x, 0x142, 0xa, 0xf, false));
  x = umax(x, __builtin_amdgcn_update_dpp(0u, x, 0x143, 0xc, 0xf, false));
  return __builtin_amdgcn_update_dpp(0u, x, 0x138, 0xf, 0xf, false);
}
DI float silu1(float v) { return v * __builtin_amdgcn_rcpf(1.0f + __builtin_amdgcn_exp2f(v * -1.4426950408889634f)); }

__global__ __launch_bounds__(512, 2) void k_main(const char* __restrict__ x, const char* __restrict__ ea, const char* __restrict__ ws, char* __restrict__ out) {
  __shared__ __attribute__((aligned(1024))) char lds_[L_END];
  asm volatile(".Lkm_code_begin:");
  LDS char* lds = (LDS char*)lds_;
  const unsigned ldsb = (unsigned)(size_t)lds;
  const int tid = threadIdx.x, l = tid & 63, w = __builtin_amdgcn_readfirstlane(tid >> 6);
  const int xcd = blockIdx.x & 7, b = xcd * 32 + (blockIdx.x >> 3);
  const int g = 8 * b + w, nbase = (g * 3125) >> 7, ncnt = (((g + 1) * 3125) >> 7) - nbase;
  const int r = l & 31, h = l >> 5;
#undef PROFBUF
#define PROFBUF ((char*)ws)
  STAMP(0);
  const auto sx = __builtin_amdgcn_make_buffer_rsrc((void*)x, 0, NN * 128, 0x00020000);
  const auto se = __builtin_amdgcn_make_buffer_rsrc((void*)ea, 0, NE * 32, 0x00020000);
  const auto sm = __builtin_amdgcn_make_buffer_rsrc((void*)(ws + WS_META), 0, NE * 4, 0x00020000);
  const auto sw = __builtin_amdgcn_make_buffer_rsrc((void*)(ws + WS_WF), 0, 34 * 1024, 0x00020000);

  const auto st = __builtin_amdgcn_make_buffer_rsrc((void*)(ws + WS_OFF), 0, NC * TROW, 0x00020000);

  const unsigned stg = L_STG + w * 5120;
  asm volatile("s_nop 4" ::: "memory");
  dma16(ldsb + L_T1 + w * 1024, (tid & 255) * TROW + b * 16 + (w >> 2) * 16, st, 0);
  const unsigned bx = blockIdx.x;
  {
    unsigned long long pc; unsigned len;
    asm volatile("s_getpc_b64 %0\n.Lkm_code_here:\n\ts_mov_b32 %1, .Lkm_code_end-.Lkm_code_here" : "=s"(pc), "=s"(len));
    const auto sc = __builtin_amdgcn_make_buffer_rsrc((void*)pc, 0, len, 0x00020000);
    const unsigned vo = l < 16 ? (w * 16 + l) * 128 : 0x7FFFFFF0u;
    asm volatile("s_cmp_lt_u32 %3, %4\n\ts_cbranch_scc0 1f\n\ts_nop 4\n\ts_mov_b32 m0, %0\n\ts_nop 0\n\tbuffer_load_dword %1, %2, 0 offen lds\n\t1:"
                 ::"s"(ldsb + L_PF + w * 256), "v"(vo), "s"(sc), "s"(bx), "n"(8 * PFW) : "m0", "scc", "memory");
  }
  const unsigned sreg = L_S + w * 2048;
  const unsigned la = L_LIST + w * 4096, lb = la + 2048;
  *(LDS u32x4*)(lds + lb + l * 16) = u32x4{512, 512, 512, 512};
  *(LDS u32x4*)(lds + lb + 1024 + l * 16) = u32x4{512, 512, 512, 512};
  f32x16 Y0, Y1;
#pragma unroll
  for (int i = 0; i < 16; ++i) { Y0[i] = 0.f; Y1[i] = 0.f; }

  unsigned xch[4];
#pragma unroll
  for (int s = 0; s < 4; ++s) xch[s] = ((l & 7) ^ (((8 * s + (l >> 3)) >> 1) & 7)) << 4;
  unsigned ech = ((l & 1) ^ ((l >> 4) & 1)) << 4;
  unsigned skey = sreg + (l >> 1) * 64 + (((l >> 3) & 3) << 4);
  unsigned aoff = la + (l >> 3) * 16;
  unsigned boff = lb + (4 * ((l >> 1) & 7) + (l >> 4)) * 4;
  unsigned xrd[4];
#pragma unroll
  for (int s = 0; s < 4; ++s) xrd[s] = stg + r * 128 + (((2 * s + h) ^ ((r >> 1) & 7)) << 4);
  unsigned erd = stg + 4096 + r * 32 + ((h ^ ((r >> 3) & 1)) << 4);
  unsigned srow0 = sreg + r * 64 + ((h ^ ((r >> 2) & 3)) << 4), srow1 = srow0 ^ 32;
  unsigned str0 = sreg + (4 * h + ((l & 15) >> 2)) * 64 + (((2 * ((l >> 4) & 1) + ((l & 3) >> 1)) ^ h) << 4) + (l & 1) * 8;
  unsigned str1 = (str0 ^ 32) + 512;

  asm volatile("" : "+v"(xch[0]), "+v"(xch[1]), "+v"(ech), "+v"(skey), "+v"(aoff), "+v"(boff), "+v"(xrd[0]), "+v"(xrd[1]), "+v"(xrd[2]), "+v"(xrd[3]));
  asm volatile("" : "+v"(erd), "+v"(srow0), "+v"(srow1), "+v"(str0), "+v"(str1), "+v"(Y0), "+v"(Y1));
  xch[2] = xch[0]; xch[3] = xch[1];
  asm volatile("s_cmp_lt_u32 %0, %1\n\ts_cbranch_scc0 1f\n\ts_waitcnt vmcnt(1)\n\ts_branch 2f\n\t1:\n\ts_waitcnt vmcnt(0)\n\t2:" ::"s"(bx), "n"(8 * PFW) : "scc", "memory");
  if (b == NB - 1 && w >= 4) *(LDS unsigned short*)(lds + L_T2 + (tid & 255) * 16) = (tid & 255) < NC ? CE : 0;
  BAR();
  STAMP(1);
  {
    const unsigned c0 = 32 * w + (l >> 3);
    unsigned s0[4], s1[4], vo[4];
#pragma unroll
    for (int i = 0; i < 4; ++i) { s0[i] = *(LDS unsigned short*)(lds + L_T1 + c0 * 16 + i * 128); s1[i] = *(LDS unsigned short*)(lds + L_T2 + c0 * 16 + i * 128); }
#pragma unroll
    for (int i = 0; i < 4; ++i) {
      const unsigned o = (s0[i] & ~3u) + 4 * (l & 7);
      vo[i] = o < s1[i] ? (__umul24(c0 + 8 * i, CE) + o) * 4 : 0x7FFFFFF0u;
    }
#pragma unroll
    for (int i = 0; i < 4; ++i) dma16(ldsb + L_M + (32 * w + 8 * i) * 128, vo[i], sm, 0);
  }

  int wb = 0, n, nw;
  unsigned cj[8], mt[8];
  auto window_scan = [&]() {
    int l = tid & 63;
    asm volatile("" : "+v"(l));
    unsigned s0[4], o0[4], o1[4], cn[4], rb[4], ix[4];
    const unsigned ta = L_T1 + l * 16, tb = ta + w * 2, tc = l * 16 + (w < 7 ? L_T1 + w * 2 + 2 : L_T2);
#pragma unroll
    for (int k = 0; k < 4; ++k) {
      s0[k] = *(LDS unsigned short*)(lds + ta + k * 1024);
      o0[k] = *(LDS unsigned short*)(lds + tb + k * 1024);
      o1[k] = *(LDS unsigned short*)(lds + tc + k * 1024);
    }
    unsigned tot = 0;
#pragma unroll
    for (int k = 0; k < 4; ++k) {
      cn[k] = o1[k] - o0[k];
      if (k == 3) cn[k] = l < NC - 192 ? cn[k] : 0u;
      rb[k] = (l << 13) + o0[k] - (s0[k] & ~3u) + ((k << 19) + 512 + (1 << 21));
      ix[k] = tot;
      tot += cn[k];
    }
    const unsigned incl = wave_incl_scan(tot);
    n = __builtin_amdgcn_readlane(incl, 63);
    nw = n - wb < CAP ? n - wb : CAP;
    const int base = (int)(incl - tot) - wb;
#pragma unroll
    for (int k = 0; k < 4; ++k) {
      const int lo = base + (int)ix[k];
      const int pos = lo > 0 ? lo : 0;
      const bool on = pos < lo + (int)cn[k] && pos < CAP;
      *(LDS unsigned*)(lds + (on ? lb + pos * 4 : la + l * 4)) = (pos << 21) + (rb[k] - lo);
    }
    asm volatile("" ::: "memory");
    const u32x4 m0 = *(LDS u32x4*)(lds + lb + l * 32), m1 = *(LDS u32x4*)(lds + lb + l * 32 + 16);
    unsigned run[8];
    run[0] = m0[0];
#pragma unroll
    for (int j = 1; j < 8; ++j) run[j] = umax(run[j - 1], j < 4 ? m0[j] : m1[j - 4]);
    const unsigned ex = wave_excl_max(run[7]);
    const int last = (nw > 0 ? nw - 1 : 0) - 512, i0 = 8 * l - 512;
#pragma unroll
    for (int j = 0; j < 8; ++j) {
      const int im = i0 < last - j ? i0 : last - j;
      cj[j] = (umax(run[j], ex) & 0x1FFFFFu) + im + j;
    }
  };
  auto window_gather = [&](bool all) {
#pragma unroll
    for (int j = 0; j < 8; ++j) {
      const unsigned c = cj[j] >> 13, q = cj[j] & 0x1FFFu;
      const unsigned s0 = *(LDS unsigned short*)(lds + L_T1 + c * 16) & ~3u;
      const unsigned v = __builtin_amdgcn_raw_buffer_load_b32(sm, (__umul24(c, CE) + s0 + q) * 4, 0, 0);
      mt[j] = (all || q >= 32) ? v : mt[j];
    }
  };
  window_scan();
  unsigned pa = L_PRG + w * 256 + l * 4;
  asm volatile("" : "+v"(pa));
  *(LDS int*)(lds + pa) = n; *(LDS int*)(lds + pa + 2048) = n;
#pragma unroll
  for (int i = 0; i < 5; ++i) {
    const int f = w + 8 * i;
    const unsigned lo = (i < 4 ? L_WIMG : L_WU - 18 * 1024) + f * 1024, hi = i < 4 ? L_WU - 18 * 1024 + f * 1024 : L_PF + 2048;
    dma16(ldsb + (i < 2 ? lo : (i == 3 ? hi : (w < 2 ? lo : hi))), l * 16, sw, (i < 4 ? f : (w < 2 ? f : 33)) * 1024);
  }
  const unsigned own = L_OWN + w * 4096;
#pragma unroll
  for (int s = 0; s < 4; ++s) {
    const int R = 8 * s + (l >> 3);
    int node = nbase + R; node = node < NN ? node : NN - 1;
    dma16(ldsb + own + s * 1024, node * 128 + (((l & 7) ^ ((R >> 1) & 7)) << 4), sx, 0);
  }

  STAMP(2);
  asm volatile("s_waitcnt vmcnt(9)" ::: "memory");
  BAR();
  {
    unsigned acc = 0;
#pragma unroll
    for (int j = 0; j < 8; ++j) {
      acc |= cj[j];
      mt[j] = *(LDS unsigned*)(lds + L_M + (((cj[j] >> 13) << 5) + (cj[j] & 31)) * 4);
    }
    const bool over = (acc & 0x1FE0u) != 0;
    BAR();
    if (__any(over)) window_gather(false);
  }
  int nt;
  u32x4 A4;
  unsigned Bq[2];
  auto dma_tile = [&](const int slot, const int tt, const u32x4 A, const unsigned B) {
    const unsigned v0 = A[0] | xch[0], v1 = A[1] | xch[1], v2 = A[2] | xch[2], v3 = A[3] | xch[3], v4 = (B & 0x07FFFFFFu) | ech;
    asm volatile("s_waitcnt lgkmcnt(0)\n\t"
                 "s_cmp_lt_i32 %[t], %[nt]\n\ts_cbranch_scc0 1f\n\t"
                 "s_mov_b32 m0, %[m]\n\ts_nop 0\n\tbuffer_load_dwordx4 %[v0], %[sx], 0 offen lds\n\t"
                 "s_add_u32 m0, %[m], 0x400\n\ts_nop 0\n\tbuffer_load_dwordx4 %[v1], %[sx], 0 offen lds\n\t"
                 "s_add_u32 m0, %[m], 0x800\n\ts_nop 0\n\tbuffer_load_dwordx4 %[v2], %[sx], 0 offen lds\n\t"
                 "s_add_u32 m0, %[m], 0xc00\n\ts_nop 0\n\tbuffer_load_dwordx4 %[v3], %[sx], 0 offen lds\n\t"
                 "s_add_u32 m0, %[m], 0x1000\n\ts_nop 0\n\tbuffer_load_dwordx4 %[v4], %[se], 0 offen " EPOL " lds\n\t"
                 "1:"
                 ::[t] "s"(tt), [nt] "s"(nt), [m] "s"(ldsb + stg + slot * SLOT), [v0] "v"(v0), [v1] "v"(v1), [v2] "v"(v2), [v3] "v"(v3), [v4] "v"(v4), [sx] "s"(sx), [se] "s"(se)
                 : "m0", "scc", "memory");
  };
  auto wait_dma = [&](const int tt) {
    asm volatile("s_cmp_lt_i32 %0, %1\n\ts_cbranch_scc1 1f\n\ts_waitcnt vmcnt(0)\n\t1:\n\ts_waitcnt vmcnt(5)" ::"s"(tt), "s"(nt) : "scc", "memory");
  };
  auto window_lists = [&]() {
    nt = (nw + 31) >> 5;
    {
      int l = tid & 63;
      asm volatile("" : "+v"(l));
      unsigned av[8], bv[8];
#pragma unroll
      for (int j = 0; j < 8; ++j) {
        const unsigned t = mt[j] >> 7, sn = __umulhi(t, 54975582u) >> 3;
        const unsigned u = (unsigned)__mul24((int)sn, -625) + t;
        const unsigned dl = __umul24(u, 41u) >> 10;
        const unsigned eh = (unsigned)__mul24((int)dl, -25) + __umul24(cj[j] >> 13, 25u) + u;
        av[j] = sn << 7;
        bv[j] = (eh << 12) | ((mt[j] << 5) & 0xFE0u) | ((8 * l + j < nw ? dl : 31u) << 27);
      }
      unsigned cl = 0;
      bool lo[8];
#pragma unroll
      for (int j = 0; j < 8; ++j) { lo[j] = av[j] < HALF * 128u && 8 * l + j < nw; cl += lo[j]; }
      const unsigned incl = wave_incl_scan(cl);
      unsigned pl = incl - cl;
      const unsigned hb = (unsigned)__builtin_amdgcn_readlane(incl, 63) + 8 * l;
#pragma unroll
      for (int j = 0; j < 8; ++j) {
        const unsigned pos = lo[j] ? pl : hb + j - pl;
        pl += lo[j];
        *(LDS unsigned*)(lds + la + pos * 4) = av[j];
        *(LDS unsigned*)(lds + la + pos * 4 + 2048) = bv[j];
      }
      asm volatile("" ::: "memory");
    }
    A4 = *(LDS u32x4*)(lds + aoff); Bq[0] = *(LDS unsigned*)(lds + boff);
    dma_tile(0, 0, A4, Bq[0]);
  };
  window_lists();
  wait_dma(0);
  BAR();
  STAMP(3);

  bf16x8 W1[4][2], W3[2], P2[2][2], XO[4];
  {
    auto wfrag = [&](int f) { return *(LDS bf16x8*)(lds + L_WIMG + f * 1024 + l * 16); };
#pragma unroll
    for (int s = 0; s < 4; ++s) { W1[s][0] = wfrag(2 * s); W1[s][1] = wfrag(2 * s + 1); }
    W3[0] = wfrag(16); W3[1] = wfrag(17);
    const int rs = (r & 19) | ((r & 4) << 1) | ((r & 8) >> 1);
    f32x16 p0, p1;
#pragma unroll
    for (int i = 0; i < 16; ++i) { p0[i] = 0.f; p1[i] = 0.f; }
#pragma unroll
    for (int s = 0; s < 4; ++s) {
      const bf16x8 a = *(LDS bf16x8*)(lds + own + rs * 128 + (((2 * s + h) ^ ((rs >> 1) & 7)) << 4));
      p0 = MFMA(a, wfrag(8 + 2 * s), p0);
      p1 = MFMA(a, wfrag(9 + 2 * s), p1);
      XO[s] = *(LDS bf16x8*)(lds + own + r * 128 + (((2 * s + h) ^ ((r >> 1) & 7)) << 4));
    }
#pragma unroll
    for (int s = 0; s < 2; ++s) {
      P2[s][0] = __builtin_bit_cast(bf16x8, u32x4{pk(p0[8 * s], p0[8 * s + 1]), pk(p0[8 * s + 2], p0[8 * s + 3]), pk(p0[8 * s + 4], p0[8 * s + 5]), pk(p0[8 * s + 6], p0[8 * s + 7])});
      P2[s][1] = __builtin_bit_cast(bf16x8, u32x4{pk(p1[8 * s], p1[8 * s + 1]), pk(p1[8 * s + 2], p1[8 * s + 3]), pk(p1[8 * s + 4], p1[8 * s + 5]), pk(p1[8 * s + 6], p1[8 * s + 7])});
    }
  }
  BAR();
  *(LDS u32x4*)(lds + sreg + l * 16) = u32x4{0, 0, 0, 0};
  *(LDS u32x4*)(lds + sreg + 1024 + l * 16) = u32x4{0, 0, 0, 0};
  while (true) {
    STAMP(4); STAMPC(0, __builtin_amdgcn_s_memtime()); STAMPC(2, nt);
    f32x16 a0, a1;
    bf16x8 xf[4], ef, sr0, sr1;
    s16x4 q00 = {0, 0, 0, 0}, q01 = q00, q10 = q00, q11 = q00;
    u32x4 mm0 = {0, 0, 0, 0}, mm1 = mm0;
    unsigned sa;
    int oth, mine;
    f32x16 zero;
#pragma unroll
    for (int i = 0; i < 16; ++i) zero[i] = 0.f;
    auto rd_frags = [&](const int slot) {
#pragma unroll
      for (int s = 0; s < 4; ++s) xf[s] = *(LDS bf16x8*)(lds + xrd[s] + slot * SLOT);
      ef = *(LDS bf16x8*)(lds + erd + slot * SLOT);
      sr0 = *(LDS bf16x8*)(lds + srow0); sr1 = *(LDS bf16x8*)(lds + srow1);
    };
    asm volatile("" ::: "memory");
    {
      const u32x4 A1 = *(LDS u32x4*)(lds + aoff + 128); Bq[1] = *(LDS unsigned*)(lds + boff + 128);
      dma_tile(1, 1, A1, Bq[1]);
      wait_dma(1);
      sa = (Bq[0] >> 26) ^ skey;
      *(LDS unsigned short*)(lds + sa) = 0x3F80;
      asm volatile("" ::: "memory");
      rd_frags(0);
      A4 = *(LDS u32x4*)(lds + aoff + 256); Bq[0] = *(LDS unsigned*)(lds + boff + 256);
      dma_tile(0, 2, A4, Bq[0]);
      a0 = MFMA(sr0, P2[0][0], zero); a0 = MFMA(sr1, P2[1][0], a0);
#pragma unroll
      for (int s = 0; s < 4; ++s) a0 = MFMA(xf[s], W1[s][0], a0);
      a0 = MFMA(ef, W3[0], a0);
    }
#define S0_ __builtin_bit_cast(bf16x8, __builtin_shufflevector(q00, q01, 0, 1, 2, 3, 4, 5, 6, 7))
#define S1_ __builtin_bit_cast(bf16x8, __builtin_shufflevector(q10, q11, 0, 1, 2, 3, 4, 5, 6, 7))
#define BF(v) __builtin_bit_cast(bf16x8, v)
#define PIN(lo, hi) _Pragma("unroll") for (int i = lo; i < hi; i += 4) asm volatile("" : "+v"(u[i]), "+v"(u[i + 1]), "+v"(u[i + 2]), "+v"(u[i + 3])); SB();
#define EXPS(lo, hi) _Pragma("unroll") for (int i = lo; i < hi; ++i) u[i] = __builtin_amdgcn_exp2f(c[i]); PIN(lo, hi)
#define ADDS(lo, hi) _Pragma("unroll") for (int i = lo; i < hi; ++i) u[i] = 1.0f + u[i]; PIN(lo, hi)
#define RCPS(lo, hi) _Pragma("unroll") for (int i = lo; i < hi; ++i) u[i] = __builtin_amdgcn_rcpf(u[i]); PIN(lo, hi)
#define FINS(lo, hi) _Pragma("unroll") for (int i = lo; i < hi; ++i) u[i] *= c[i]; PIN(lo, hi)
    auto phase = [&](const int T, const int par, const int t, f32x16& c, f32x16& nx, f32x16& Yc, f32x16& Yp) {
      float u[16];
      SB();
      Yp = MFMA(BF(mm1), S1_, Yp); SB();
      if (T == 0) {
        q00 = __builtin_amdgcn_ds_read_tr16_b64_v4i16((LDS s16x4*)(lds + str0));
        q01 = __builtin_amdgcn_ds_read_tr16_b64_v4i16((LDS s16x4*)(lds + str1));
        q10 = __builtin_amdgcn_ds_read_tr16_b64_v4i16((LDS s16x4*)(lds + str0 + 1024));
        q11 = __builtin_amdgcn_ds_read_tr16_b64_v4i16((LDS s16x4*)(lds + str1 + 1024));
        asm volatile("" ::: "memory");
        *(LDS unsigned short*)(lds + sa) = 0;
        sa = (Bq[par ^ 1] >> 26) ^ skey;
        SB();
      } else {
        dma_tile(par ^ 1, t + 3, A4, Bq[par ^ 1]);
        SB();
      }
      const int U = T ^ 1;
      nx = MFMA(sr0, P2[0][U], zero); SB();
      EXPS(0, 4)
      nx = MFMA(sr1, P2[1][U], nx); SB();
      EXPS(4, 8)
      nx = MFMA(xf[0], W1[0][U], nx); SB();
      EXPS(8, 12)
      nx = MFMA(xf[1], W1[1][U], nx); SB();
      EXPS(12, 16)
      nx = MFMA(xf[2], W1[2][U], nx); SB();
      ADDS(0, 16)
      nx = MFMA(xf[3], W1[3][U], nx); SB();
      RCPS(0, 4)
      nx = MFMA(ef, W3[U], nx); SB();
      if (T == 0) {
        wait_dma(t + 2);
        *(LDS unsigned short*)(lds + sa) = 0x3F80;
        asm volatile("" ::: "memory");
        rd_frags(par ^ 1);
        A4 = *(LDS u32x4*)(lds + aoff + (t + 3) * 128);
        Bq[par ^ 1] = *(LDS unsigned*)(lds + boff + (t + 3) * 128);
        const int left = n - wb - 32 * t;
        *(LDS int*)(lds + pa) = left; *(LDS int*)(lds + pa + 2048) = left;
        oth = *(LDS int*)(lds + pa + 1024);
        SB();
      }
      if (T == 1) {
        const int o = __builtin_amdgcn_readfirstlane(oth), left = __builtin_amdgcn_readfirstlane(n - wb - 32 * t);
        asm volatile("s_cmp_ge_i32 %0, %1\n\ts_cbranch_scc1 1f\n\ts_setprio 0\n\ts_branch 2f\n\t1:\n\ts_setprio 1\n\t2:" ::"s"(left), "s"(o) : "scc");
        SB();
      }
      RCPS(4, 16)
      FINS(0, 16)
      mm0 = u32x4{pk(u[0], u[1]), pk(u[2], u[3]), pk(u[4], u[5]), pk(u[6], u[7])}; SB();
      mm1 = u32x4{pk(u[8], u[9]), pk(u[10], u[11]), pk(u[12], u[13]), pk(u[14], u[15])}; SB();
      Yc = MFMA(BF(mm0), S0_, Yc); SB();
    };
    for (int t = 0; t < nt; t += 2) {
      phase(0, 0, t, a0, a1, Y0, Y1);
      phase(1, 0, t, a1, a0, Y1, Y0);
      if (t + 1 >= nt) break;
      phase(0, 1, t + 1, a0, a1, Y0, Y1);
      phase(1, 1, t + 1, a1, a0, Y1, Y0);
    }
    Y1 = MFMA(BF(mm1), S1_, Y1);
    *(LDS unsigned short*)(lds + sa) = 0;
    wb += CAP;
    if (wb >= n) break;
    *(LDS u32x4*)(lds + lb + (tid & 63) * 16) = u32x4{512, 512, 512, 512};
    *(LDS u32x4*)(lds + lb + 1024 + (tid & 63) * 16) = u32x4{512, 512, 512, 512};
    asm volatile("" ::: "memory");
    window_scan();
    window_gather(true);
    window_lists();
  }

  STAMP(5); STAMPC(1, __builtin_amdgcn_s_memtime());
#pragma unroll
  for (int i = 0; i < 16; ++i) { Y0[i] *= -0.6931471805599453f; Y1[i] *= -0.6931471805599453f; }
  f32x16 o0, o1;
#pragma unroll
  for (int i = 0; i < 16; ++i) { o0[i] = 0.f; o1[i] = 0.f; }
  auto ufrag = [&](int f) { return *(LDS bf16x8*)(lds + L_WU + f * 1024 + l * 16); };
#pragma unroll
  for (int s = 0; s < 4; ++s) { o0 = MFMA(ufrag(2 * s), XO[s], o0); o1 = MFMA(ufrag(2 * s + 1), XO[s], o1); }
#pragma unroll
  for (int s = 0; s < 2; ++s) {
    const bf16x8 z0 = __builtin_bit_cast(bf16x8, u32x4{pk(Y0[8 * s], Y0[8 * s + 1]), pk(Y0[8 * s + 2], Y0[8 * s + 3]), pk(Y0[8 * s + 4], Y0[8 * s + 5]), pk(Y0[8 * s + 6], Y0[8 * s + 7])});
    const bf16x8 z1 = __builtin_bit_cast(bf16x8, u32x4{pk(Y1[8 * s], Y1[8 * s + 1]), pk(Y1[8 * s + 2], Y1[8 * s + 3]), pk(Y1[8 * s + 4], Y1[8 * s + 5]), pk(Y1[8 * s + 6], Y1[8 * s + 7])});
    o0 = MFMA(ufrag(8 + 2 * s), z0, o0); o1 = MFMA(ufrag(9 + 2 * s), z0, o1);
    o0 = MFMA(ufrag(12 + 2 * s), z1, o0); o1 = MFMA(ufrag(13 + 2 * s), z1, o1);
  }
#pragma unroll
  for (int q = 0; q < 4; ++q) {
    const u32x2 v0 = {pk(silu1(o0[4 * q]), silu1(o0[4 * q + 1])), pk(silu1(o0[4 * q + 2]), silu1(o0[4 * q + 3]))};
    const u32x2 v1 = {pk(silu1(o1[4 * q]), silu1(o1[4 * q + 1])), pk(silu1(o1[4 * q + 2]), silu1(o1[4 * q + 3]))};
    *(LDS u32x2*)(lds + stg + r * 128 + ((q ^ (r & 7)) << 4) + h * 8) = v0;
    *(LDS u32x2*)(lds + stg + r * 128 + (((4 + q) ^ (r & 7)) << 4) + h * 8) = v1;
  }
  asm volatile("" ::: "memory");
#pragma unroll
  for (int it = 0; it < 4; ++it) {
    const int m = l + 64 * it, row = m >> 3, p = m & 7;
    if (m < ncnt * 8) {
      const u32x4 v = *(LDS u32x4*)(lds + stg + row * 128 + ((p ^ (row & 7)) << 4));
      *(u32x4*)(out + (size_t)nbase * 128 + m * 16) = v;
    }
  }
  STAMP(6);
  asm volatile(".Lkm_code_end:");
}

static inline void launch_part(void* const* d_in, void* d_ws, hipStream_t stream) {
  k_part<<<NC + 5, 512, 0, stream>>>((const i32x4*)d_in[3], (const i32x4*)d_in[2], (const u32x4*)d_in[0], (const uint16_t*)d_in[4], (const uint16_t*)d_in[5], (char*)d_ws);
}
static inline void launch_main(void* const* d_in, void* d_out, void* d_ws, hipStream_t stream) {
  k_main<<<NB, 512, 0, stream>>>((const char*)d_in[0], (const char*)d_in[1], (const char*)d_ws, (char*)d_out);
}

extern "C" void kernel_launch(void* const* d_in, const int* in_sizes, int n_in, void* d_out, int out_size, void* d_ws, size_t ws_size,
                              hipStream_t stream) {
  launch_part(d_in, d_ws, stream);
  launch_main(d_in, d_out, d_ws, stream);
}
```

```cpp
#include <hip/hip_runtime.h>
#include <stdint.h>

#define LDS __attribute__((address_space(3)))
#define DI __device__ __forceinline__
typedef float f32x16 __attribute__((ext_vector_type(16)));
typedef float f32x2 __attribute__((ext_vector_type(2)));
typedef unsigned u32x4 __attribute__((ext_vector_type(4)));
typedef unsigned u32x2 __attribute__((ext_vector_type(2)));
typedef int i32x4 __attribute__((ext_vector_type(4)));
typedef __bf16 bf16x8 __attribute__((ext_vector_type(8)));
typedef __bf16 bf16x2 __attribute__((ext_vector_type(2)));
typedef short s16x4 __attribute__((ext_vector_type(4)));

constexpr int NN = 50000;
constexpr int NE = 800000;
constexpr int GP = 25;
constexpr int CE = 3200;
constexpr int NC = 250;
constexpr int NB = 256;
constexpr int TROW = 4096;
#define HALF 25000
constexpr int CAP = 512;
constexpr unsigned WS_EP = 0, WS_META = 25600000u, WS_OFF = 28800000u, WS_WF = 29824000u;

#define MFMA(a, b, c) __builtin_amdgcn_mfma_f32_32x32x16_bf16((a), (b), (c), 0, 0, 0)
#define BAR() asm volatile("s_waitcnt lgkmcnt(0)\n\ts_barrier" ::: "memory")
#define SB() __builtin_amdgcn_sched_barrier(0)
#define EPOL "nt"
#define STAMP(k)
#define STAMPC(k, v)

DI unsigned pk(float a, float b) { f32x2 v = {a, b}; bf16x2 r = __builtin_convertvector(v, bf16x2); return __builtin_bit_cast(unsigned, r); }
DI unsigned wave_incl_scan(unsigned x) {
  x += __builtin_amdgcn_update_dpp(0u, x, 0x111, 0xf, 0xf, false);
  x += __builtin_amdgcn_update_dpp(0u, x, 0x112, 0xf, 0xf, false);
  x += __builtin_amdgcn_update_dpp(0u, x, 0x114, 0xf, 0xf, false);
  x += __builtin_amdgcn_update_dpp(0u, x, 0x118, 0xf, 0xf, false);
  x += __builtin_amdgcn_update_dpp(0u, x, 0x142, 0xa, 0xf, false);
  x += __builtin_amdgcn_update_dpp(0u, x, 0x143, 0xc, 0xf, false);
  return x;
}

__global__ __launch_bounds__(512) void k_part(const i32x4* __restrict__ dst4, const i32x4* __restrict__ src4, const u32x4* __restrict__ x16,
                                              const uint16_t* __restrict__ wm, const uint16_t* __restrict__ wu, char* __restrict__ ws) {
  __shared__ __attribute__((aligned(16))) unsigned cnt[2048];
  __shared__ __attribute__((aligned(16))) unsigned metaL[CE];
  __shared__ unsigned wsum[8];
  const int tid = threadIdx.x, c = blockIdx.x;
#undef PROFBUF
#define PROFBUF (ws + (8u << 20))
  if (c >= NC) {
    const int it = (c - NC) * 512 + tid;
    if (it < 34 * 64) {
      const int f = it >> 6, l = it & 63, col = l & 31, h = l >> 5;
      unsigned short v[8];
#pragma unroll
      for (int j = 0; j < 8; ++j) {
        if (f < 18) {
          const int t = f & 1, s = (f >> 1) & 3;
          const int row = (f < 8 ? 0 : (f < 16 ? 64 : 128)) + (f < 16 ? 16 * s : 0) + 8 * h + j;
          v[j] = wm[row * 64 + 32 * t + col];
        } else {
          const int ks = (f - 18) >> 1, u = f & 1;
          const int row = ks < 4 ? 16 * ks + 8 * h + j : 64 + 32 * ((ks - 4) >> 1) + 16 * (ks & 1) + 8 * (j >> 2) + 4 * h + (j & 3);
          v[j] = wu[row * 64 + 32 * u + col];
        }
      }
      u32x4 o = {v[0] | ((unsigned)v[1] << 16), v[2] | ((unsigned)v[3] << 16), v[4] | ((unsigned)v[5] << 16), v[6] | ((unsigned)v[7] << 16)};
      if (f < 18) {
#pragma unroll
        for (int q = 0; q < 4; ++q)
          o[q] = pk(__builtin_bit_cast(float, o[q] << 16) * -1.4426950408889634f, __builtin_bit_cast(float, o[q] & 0xFFFF0000u) * -1.4426950408889634f);
      }
      *(u32x4*)(ws + WS_WF + it * 16) = o;
    }
    return;
  }
  STAMP(0);
  constexpr int TA = CE / 8;
  i32x4 d[2], s[2];
  const int ta = tid < TA ? tid : TA - 1;
#pragma unroll
  for (int i = 0; i < 2; ++i) { d[i] = dst4[c * (CE / 4) + ta + i * TA]; s[i] = src4[c * (CE / 4) + ta + i * TA]; }
  unsigned cw[2] = {0, 0};
  if (c < 8 && __builtin_amdgcn_readfirstlane(tid >> 6) == 7) {
    unsigned long long pc; int off; unsigned len;
    asm volatile("s_getpc_b64 %0\n.Lkp_code_here:\n\ts_mov_b32 %1, .Lkm_code_begin-.Lkp_code_here\n\ts_mov_b32 %2, .Lkm_code_end-.Lkm_code_begin" : "=s"(pc), "=s"(off), "=s"(len));
    typedef __attribute__((address_space(1))) const unsigned* gptr;
    const unsigned long long base = (pc + off) & ~255ull;
#pragma unroll
    for (int i = 0; i < 2; ++i) { const unsigned o = (i * 64 + (tid & 63)) * 128; cw[i] = *(gptr)(base + (o < len - 4 ? o : len - 4)); }
  }
  *(u32x4*)&cnt[4 * tid] = u32x4{0, 0, 0, 0};
  BAR();
  STAMP(1);
  unsigned g[8], rk[8];
  if (tid < TA) {
#pragma unroll
    for (int j = 0; j < 8; ++j) { g[j] = ((unsigned)d[j >> 2][j & 3] * 128u + 127u) / 3125u; rk[j] = __hip_atomic_fetch_add(&cnt[g[j]], 1u, __ATOMIC_RELAXED, __HIP_MEMORY_SCOPE_WORKGROUP); }
  }
  BAR();
  STAMP(2);
  const u32x4 cc = *(const u32x4*)&cnt[4 * tid];
  const unsigned tot = cc[0] + cc[1] + cc[2] + cc[3];
  const unsigned incl = wave_incl_scan(tot);
  if ((tid & 63) == 63) wsum[tid >> 6] = incl;
  BAR();
  unsigned ws8 = wsum[tid & 7];
  ws8 += __builtin_amdgcn_update_dpp(0u, ws8, 0x111, 0xf, 0xf, false);
  ws8 += __builtin_amdgcn_update_dpp(0u, ws8, 0x112, 0xf, 0xf, false);
  ws8 += __builtin_amdgcn_update_dpp(0u, ws8, 0x114, 0xf, 0xf, false);
  const int w = __builtin_amdgcn_readfirstlane(tid >> 6);
  const unsigned wb = w ? __builtin_amdgcn_readlane(ws8, w - 1) : 0u;
  const unsigned o0 = wb + incl - tot, o1 = o0 + cc[0], o2 = o1 + cc[1], o3 = o2 + cc[2];
  *(u32x4*)&cnt[4 * tid] = u32x4{o0, o1, o2, o3};
  *(u32x2*)(ws + WS_OFF + c * TROW + tid * 8) = u32x2{o0 | (o1 << 16), o2 | (o3 << 16)};
  BAR();
  STAMP(3);
  if (tid < TA) {
#pragma unroll
    for (int j = 0; j < 8; ++j) {
      const unsigned dv = d[j >> 2][j & 3], sv = s[j >> 2][j & 3];
      metaL[cnt[g[j]] + rk[j]] = sv * (GP * CE) + (dv - (__umul24(g[j], 3125u) >> 7)) * CE + (4 * tid + (j & 3) + (j >> 2) * (4 * TA));
    }
  }
  BAR();
  STAMP(4);
  if (tid < TA) {
#pragma unroll
    for (int i = 0; i < 2; ++i) *(u32x4*)(ws + WS_META + c * (CE * 4) + (tid + i * TA) * 16) = *(const u32x4*)&metaL[4 * (tid + i * TA)];
  }
  asm volatile("" ::"v"(cw[0]), "v"(cw[1]));
  STAMP(5);
  STAMP(6);
}

constexpr unsigned L_STG = 0;
constexpr unsigned SLOT = 40960;
constexpr unsigned L_S = 81920;
constexpr unsigned L_LIST = 98304;
constexpr unsigned L_WU = 131072;
constexpr unsigned L_T1 = 147456;
constexpr unsigned L_T2 = 151552;
constexpr unsigned L_PRG = 155648;
constexpr unsigned L_END = 158720;
constexpr unsigned L_M = 0;
constexpr unsigned L_OWN = 40960;
constexpr unsigned L_WIMG = 73728;
constexpr unsigned L_PF = 92160;
#define PFW 1

DI void dma16(unsigned m0, unsigned voff, __amdgpu_buffer_rsrc_t srd, unsigned soff) {
  asm volatile("s_mov_b32 m0, %0\n\ts_nop 0\n\tbuffer_load_dwordx4 %1, %2, %3 offen lds" ::"s"(m0), "v"(voff), "s"(srd), "s"(soff) : "m0", "memory");
}
DI unsigned umax(unsigned a, unsigned b) { return a > b ? a : b; }
DI unsigned wave_excl_max(unsigned x) {
  x = umax(x, __builtin_amdgcn_update_dpp(0u, x, 0x111, 0xf, 0xf, false));
  x = umax(x, __builtin_amdgcn_update_dpp(0u, x, 0x112, 0xf, 0xf, false));
  x = umax(x, __builtin_amdgcn_update_dpp(0u, x, 0x114, 0xf, 0xf, false));
  x = umax(x, __builtin_amdgcn_update_dpp(0u, x, 0x118, 0xf, 0xf, false));
  x = umax(x, __builtin_amdgcn_update_dpp(0u, x, 0x142, 0xa, 0xf, false));
  x = umax(x, __builtin_amdgcn_update_dpp(0u, x, 0x143, 0xc, 0xf, false));
  return __builtin_amdgcn_update_dpp(0u, x, 0x138, 0xf, 0xf, false);
}
DI float silu1(float v) { return v * __builtin_amdgcn_rcpf(1.0f + __builtin_amdgcn_exp2f(v * -1.4426950408889634f)); }

__global__ __launch_bounds__(512, 2) void k_main(const char* __restrict__ x, const char* __restrict__ ea, const char* __restrict__ ws, char* __restrict__ out) {
  __shared__ __attribute__((aligned(1024))) char lds_[L_END];
  asm volatile(".Lkm_code_begin:");
  LDS char* lds = (LDS char*)lds_;
  const unsigned ldsb = (unsigned)(size_t)lds;
  const int tid = threadIdx.x, l = tid & 63, w = __builtin_amdgcn_readfirstlane(tid >> 6);
  const int xcd = blockIdx.x & 7, b = xcd * 32 + (blockIdx.x >> 3);
  const int g = 8 * b + w, nbase = (g * 3125) >> 7, ncnt = (((g + 1) * 3125) >> 7) - nbase;
  const int r = l & 31, h = l >> 5;
#undef PROFBUF
#define PROFBUF ((char*)ws)
  STAMP(0);
  const auto sx = __builtin_amdgcn_make_buffer_rsrc((void*)x, 0, NN * 128, 0x00020000);
  const auto se = __builtin_amdgcn_make_buffer_rsrc((void*)ea, 0, NE * 32, 0x00020000);
  const auto sm = __builtin_amdgcn_make_buffer_rsrc((void*)(ws + WS_META), 0, NE * 4, 0x00020000);
  const auto sw = __builtin_amdgcn_make_buffer_rsrc((void*)(ws + WS_WF), 0, 34 * 1024, 0x00020000);

  const auto st = __builtin_amdgcn_make_buffer_rsrc((void*)(ws + WS_OFF), 0, NC * TROW, 0x00020000);

  const unsigned stg = L_STG + w * 5120;
  asm volatile("s_nop 4" ::: "memory");
  dma16(ldsb + L_T1 + w * 1024, (tid & 255) * TROW + b * 16 + (w >> 2) * 16, st, 0);
  const unsigned bx = blockIdx.x;
  {
    unsigned long long pc; unsigned len;
    asm volatile("s_getpc_b64 %0\n.Lkm_code_here:\n\ts_mov_b32 %1, .Lkm_code_end-.Lkm_code_here" : "=s"(pc), "=s"(len));
    const auto sc = __builtin_amdgcn_make_buffer_rsrc((void*)pc, 0, len, 0x00020000);
    const unsigned vo = l < 16 ? (w * 16 + l) * 128 : 0x7FFFFFF0u;
    asm volatile("s_cmp_lt_u32 %3, %4\n\ts_cbranch_scc0 1f\n\ts_nop 4\n\ts_mov_b32 m0, %0\n\ts_nop 0\n\tbuffer_load_dword %1, %2, 0 offen lds\n\t1:"
                 ::"s"(ldsb + L_PF + w * 256), "v"(vo), "s"(sc), "s"(bx), "n"(8 * PFW) : "m0", "scc", "memory");
    const auto sp = __builtin_amdgcn_make_buffer_rsrc((void*)(w < 6 ? ea : x), 0, w < 6 ? NE * 32 : NN * 128, 0x00020000);
    const unsigned vt = w < 6 ? __umul24(tid, 66688u) : __umul24(tid - 384, 50000u);
    asm volatile("s_cmp_lt_u32 %3, %4\n\ts_cbranch_scc0 1f\n\ts_nop 4\n\ts_mov_b32 m0, %0\n\ts_nop 0\n\tbuffer_load_dword %1, %2, 0 offen lds\n\t1:"
                 ::"s"(ldsb + L_PF + w * 256), "v"(vt), "s"(sp), "s"(bx), "n"(8 * PFW) : "m0", "scc", "memory");
  }
  const unsigned sreg = L_S + w * 2048;
  const unsigned la = L_LIST + w * 4096, lb = la + 2048;
  *(LDS u32x4*)(lds + lb + l * 16) = u32x4{512, 512, 512, 512};
  *(LDS u32x4*)(lds + lb + 1024 + l * 16) = u32x4{512, 512, 512, 512};
  f32x16 Y0, Y1;
#pragma unroll
  for (int i = 0; i < 16; ++i) { Y0[i] = 0.f; Y1[i] = 0.f; }

  unsigned xch[4];
#pragma unroll
  for (int s = 0; s < 4; ++s) xch[s] = ((l & 7) ^ (((8 * s + (l >> 3)) >> 1) & 7)) << 4;
  unsigned ech = ((l & 1) ^ ((l >> 4) & 1)) << 4;
  unsigned skey = sreg + (l >> 1) * 64 + (((l >> 3) & 3) << 4);
  unsigned aoff = la + (l >> 3) * 16;
  unsigned boff = lb + (4 * ((l >> 1) & 7) + (l >> 4)) * 4;
  unsigned xrd[4];
#pragma unroll
  for (int s = 0; s < 4; ++s) xrd[s] = stg + r * 128 + (((2 * s + h) ^ ((r >> 1) & 7)) << 4);
  unsigned erd = stg + 4096 + r * 32 + ((h ^ ((r >> 3) & 1)) << 4);
  unsigned srow0 = sreg + r * 64 + ((h ^ ((r >> 2) & 3)) << 4), srow1 = srow0 ^ 32;
  unsigned str0 = sreg + (4 * h + ((l & 15) >> 2)) * 64 + (((2 * ((l >> 4) & 1) + ((l & 3) >> 1)) ^ h) << 4) + (l & 1) * 8;
  unsigned str1 = (str0 ^ 32) + 512;

  asm volatile("" : "+v"(xch[0]), "+v"(xch[1]), "+v"(ech), "+v"(skey), "+v"(aoff), "+v"(boff), "+v"(xrd[0]), "+v"(xrd[1]), "+v"(xrd[2]), "+v"(xrd[3]));
  asm volatile("" : "+v"(erd), "+v"(srow0), "+v"(srow1), "+v"(str0), "+v"(str1), "+v"(Y0), "+v"(Y1));
  xch[2] = xch[0]; xch[3] = xch[1];
  asm volatile("s_cmp_lt_u32 %0, %1\n\ts_cbranch_scc0 1f\n\ts_waitcnt vmcnt(2)\n\ts_branch 2f\n\t1:\n\ts_waitcnt vmcnt(0)\n\t2:" ::"s"(bx), "n"(8 * PFW) : "scc", "memory");
  if (b == NB - 1 && w >= 4) *(LDS unsigned short*)(lds + L_T2 + (tid & 255) * 16) = (tid & 255) < NC ? CE : 0;
  BAR();
  STAMP(1);
  {
    const unsigned c0 = 32 * w + (l >> 3);
    unsigned s0[4], s1[4], vo[4];
#pragma unroll
    for (int i = 0; i < 4; ++i) { s0[i] = *(LDS unsigned short*)(lds + L_T1 + c0 * 16 + i * 128); s1[i] = *(LDS unsigned short*)(lds + L_T2 + c0 * 16 + i * 128); }
#pragma unroll
    for (int i = 0; i < 4; ++i) {
      const unsigned o = (s0[i] & ~3u) + 4 * (l & 7);
      vo[i] = o < s1[i] ? (__umul24(c0 + 8 * i, CE) + o) * 4 : 0x7FFFFFF0u;
    }
#pragma unroll
    for (int i = 0; i < 4; ++i) dma16(ldsb + L_M + (32 * w + 8 * i) * 128, vo[i], sm, 0);
  }

  int wb = 0, n, nw;
  unsigned cj[8], mt[8];
  auto window_scan = [&]() {
    int l = tid & 63;
    asm volatile("" : "+v"(l));
    unsigned s0[4], o0[4], o1[4], cn[4], rb[4], ix[4];
    const unsigned ta = L_T1 + l * 16, tb = ta + w * 2, tc = l * 16 + (w < 7 ? L_T1 + w * 2 + 2 : L_T2);
#pragma unroll
    for (int k = 0; k < 4; ++k) {
      s0[k] = *(LDS unsigned short*)(lds + ta + k * 1024);
      o0[k] = *(LDS unsigned short*)(lds + tb + k * 1024);
      o1[k] = *(LDS unsigned short*)(lds + tc + k * 1024);
    }
    unsigned tot = 0;
#pragma unroll
    for (int k = 0; k < 4; ++k) {
      cn[k] = o1[k] - o0[k];
      if (k == 3) cn[k] = l < NC - 192 ? cn[k] : 0u;
      rb[k] = (l << 13) + o0[k] - (s0[k] & ~3u) + ((k << 19) + 512 + (1 << 21));
      ix[k] = tot;
      tot += cn[k];
    }
    const unsigned incl = wave_incl_scan(tot);
    n = __builtin_amdgcn_readlane(incl, 63);
    nw = n - wb < CAP ? n - wb : CAP;
    const int base = (int)(incl - tot) - wb;
#pragma unroll
    for (int k = 0; k < 4; ++k) {
      const int lo = base + (int)ix[k];
      const int pos = lo > 0 ? lo : 0;
      const bool on = pos < lo + (int)cn[k] && pos < CAP;
      *(LDS unsigned*)(lds + (on ? lb + pos * 4 : la + l * 4)) = (pos << 21) + (rb[k] - lo);
    }
    asm volatile("" ::: "memory");
    const u32x4 m0 = *(LDS u32x4*)(lds + lb + l * 32), m1 = *(LDS u32x4*)(lds + lb + l * 32 + 16);
    unsigned run[8];
    run[0] = m0[0];
#pragma unroll
    for (int j = 1; j < 8; ++j) run[j] = umax(run[j - 1], j < 4 ? m0[j] : m1[j - 4]);
    const unsigned ex = wave_excl_max(run[7]);
    const int last = (nw > 0 ? nw - 1 : 0) - 512, i0 = 8 * l - 512;
#pragma unroll
    for (int j = 0; j < 8; ++j) {
      const int im = i0 < last - j ? i0 : last - j;
      cj[j] = (umax(run[j], ex) & 0x1FFFFFu) + im + j;
    }
  };
  auto window_gather = [&](bool all) {
#pragma unroll
    for (int j = 0; j < 8; ++j) {
      const unsigned c = cj[j] >> 13, q = cj[j] & 0x1FFFu;
      const unsigned s0 = *(LDS unsigned short*)(lds + L_T1 + c * 16) & ~3u;
      const unsigned v = __builtin_amdgcn_raw_buffer_load_b32(sm, (__umul24(c, CE) + s0 + q) * 4, 0, 0);
      mt[j] = (all || q >= 32) ? v : mt[j];
    }
  };
  window_scan();
  unsigned pa = L_PRG + w * 256 + l * 4;
  asm volatile("" : "+v"(pa));
  *(LDS int*)(lds + pa) = n; *(LDS int*)(lds + pa + 2048) = n;
#pragma unroll
  for (int i = 0; i < 5; ++i) {
    const int f = w + 8 * i;
    const unsigned lo = (i < 4 ? L_WIMG : L_WU - 18 * 1024) + f * 1024, hi = i < 4 ? L_WU - 18 * 1024 + f * 1024 : L_PF + 2048;
    dma16(ldsb + (i < 2 ? lo : (i == 3 ? hi : (w < 2 ? lo : hi))), l * 16, sw, (i < 4 ? f : (w < 2 ? f : 33)) * 1024);
  }
  const unsigned own = L_OWN + w * 4096;
#pragma unroll
  for (int s = 0; s < 4; ++s) {
    const int R = 8 * s + (l >> 3);
    int node = nbase + R; node = node < NN ? node : NN - 1;
    dma16(ldsb + own + s * 1024, node * 128 + (((l & 7) ^ ((R >> 1) & 7)) << 4), sx, 0);
  }

  STAMP(2);
  asm volatile("s_waitcnt vmcnt(9)" ::: "memory");
  BAR();
  {
    unsigned acc = 0;
#pragma unroll
    for (int j = 0; j < 8; ++j) {
      acc |= cj[j];
      mt[j] = *(LDS unsigned*)(lds + L_M + (((cj[j] >> 13) << 5) + (cj[j] & 31)) * 4);
    }
    const bool over = (acc & 0x1FE0u) != 0;
    BAR();
    if (__any(over)) window_gather(false);
  }
  int nt;
  u32x4 A4;
  unsigned Bq[2];
  auto dma_tile = [&](const int slot, const int tt, const u32x4 A, const unsigned B) {
    const unsigned v0 = A[0] | xch[0], v1 = A[1] | xch[1], v2 = A[2] | xch[2], v3 = A[3] | xch[3], v4 = (B & 0x07FFFFFFu) | ech;
    asm volatile("s_waitcnt lgkmcnt(0)\n\t"
                 "s_cmp_lt_i32 %[t], %[nt]\n\ts_cbranch_scc0 1f\n\t"
                 "s_mov_b32 m0, %[m]\n\ts_nop 0\n\tbuffer_load_dwordx4 %[v0], %[sx], 0 offen lds\n\t"
                 "s_add_u32 m0, %[m], 0x400\n\ts_nop 0\n\tbuffer_load_dwordx4 %[v1], %[sx], 0 offen lds\n\t"
                 "s_add_u32 m0, %[m], 0x800\n\ts_nop 0\n\tbuffer_load_dwordx4 %[v2], %[sx], 0 offen lds\n\t"
                 "s_add_u32 m0, %[m], 0xc00\n\ts_nop 0\n\tbuffer_load_dwordx4 %[v3], %[sx], 0 offen lds\n\t"
                 "s_add_u32 m0, %[m], 0x1000\n\ts_nop 0\n\tbuffer_load_dwordx4 %[v4], %[se], 0 offen " EPOL " lds\n\t"
                 "1:"
                 ::[t] "s"(tt), [nt] "s"(nt), [m] "s"(ldsb + stg + slot * SLOT), [v0] "v"(v0), [v1] "v"(v1), [v2] "v"(v2), [v3] "v"(v3), [v4] "v"(v4), [sx] "s"(sx), [se] "s"(se)
                 : "m0", "scc", "memory");
  };
  auto wait_dma = [&](const int tt) {
    asm volatile("s_cmp_lt_i32 %0, %1\n\ts_cbranch_scc1 1f\n\ts_waitcnt vmcnt(0)\n\t1:\n\ts_waitcnt vmcnt(5)" ::"s"(tt), "s"(nt) : "scc", "memory");
  };
  auto window_lists = [&]() {
    nt = (nw + 31) >> 5;
    {
      int l = tid & 63;
      asm volatile("" : "+v"(l));
      unsigned av[8], bv[8];
#pragma unroll
      for (int j = 0; j < 8; ++j) {
        const unsigned t = mt[j] >> 7, sn = __umulhi(t, 54975582u) >> 3;
        const unsigned u = (unsigned)__mul24((int)sn, -625) + t;
        const unsigned dl = __umul24(u, 41u) >> 10;
        const unsigned eh = (unsigned)__mul24((int)dl, -25) + __umul24(cj[j] >> 13, 25u) + u;
        av[j] = sn << 7;
        bv[j] = (eh << 12) | ((mt[j] << 5) & 0xFE0u) | ((8 * l + j < nw ? dl : 31u) << 27);
      }
      unsigned cl = 0;
      bool lo[8];
#pragma unroll
      for (int j = 0; j < 8; ++j) { lo[j] = (av[j] < HALF * 128u) != (b >= NB / 2) && 8 * l + j < nw; cl += lo[j]; }
      const unsigned incl = wave_incl_scan(cl);
      unsigned pl = incl - cl;
      const unsigned hb = (unsigned)__builtin_amdgcn_readlane(incl, 63) + 8 * l;
#pragma unroll
      for (int j = 0; j < 8; ++j) {
        const unsigned pos = lo[j] ? pl : hb + j - pl;
        pl += lo[j];
        *(LDS unsigned*)(lds + la + pos * 4) = av[j];
        *(LDS unsigned*)(lds + la + pos * 4 + 2048) = bv[j];
      }
      asm volatile("" ::: "memory");
    }
    A4 = *(LDS u32x4*)(lds + aoff); Bq[0] = *(LDS unsigned*)(lds + boff);
    dma_tile(0, 0, A4, Bq[0]);
  };
  window_lists();
  wait_dma(0);
  BAR();
  STAMP(3);

  bf16x8 W1[4][2], W3[2], P2[2][2], XO[4];
  {
    auto wfrag = [&](int f) { return *(LDS bf16x8*)(lds + L_WIMG + f * 1024 + l * 16); };
#pragma unroll
    for (int s = 0; s < 4; ++s) { W1[s][0] = wfrag(2 * s); W1[s][1] = wfrag(2 * s + 1); }
    W3[0] = wfrag(16); W3[1] = wfrag(17);
    const int rs = (r & 19) | ((r & 4) << 1) | ((r & 8) >> 1);
    f32x16 p0, p1;
#pragma unroll
    for (int i = 0; i < 16; ++i) { p0[i] = 0.f; p1[i] = 0.f; }
#pragma unroll
    for (int s = 0; s < 4; ++s) {
      const bf16x8 a = *(LDS bf16x8*)(lds + own + rs * 128 + (((2 * s + h) ^ ((rs >> 1) & 7)) << 4));
      p0 = MFMA(a, wfrag(8 + 2 * s), p0);
      p1 = MFMA(a, wfrag(9 + 2 * s), p1);
      XO[s] = *(LDS bf16x8*)(lds + own + r * 128 + (((2 * s + h) ^ ((r >> 1) & 7)) << 4));
    }
#pragma unroll
    for (int s = 0; s < 2; ++s) {
      P2[s][0] = __builtin_bit_cast(bf16x8, u32x4{pk(p0[8 * s], p0[8 * s + 1]), pk(p0[8 * s + 2], p0[8 * s + 3]), pk(p0[8 * s + 4], p0[8 * s + 5]), pk(p0[8 * s + 6], p0[8 * s + 7])});
      P2[s][1] = __builtin_bit_cast(bf16x8, u32x4{pk(p1[8 * s], p1[8 * s + 1]), pk(p1[8 * s + 2], p1[8 * s + 3]), pk(p1[8 * s + 4], p1[8 * s + 5]), pk(p1[8 * s + 6], p1[8 * s + 7])});
    }
  }
  BAR();
  *(LDS u32x4*)(lds + sreg + l * 16) = u32x4{0, 0, 0, 0};
  *(LDS u32x4*)(lds + sreg + 1024 + l * 16) = u32x4{0, 0, 0, 0};
  while (true) {
    STAMP(4); STAMPC(0, __builtin_amdgcn_s_memtime()); STAMPC(2, nt);
    f32x16 a0, a1;
    bf16x8 xf[4], ef, sr0, sr1;
    s16x4 q00 = {0, 0, 0, 0}, q01 = q00, q10 = q00, q11 = q00;
    u32x4 mm0 = {0, 0, 0, 0}, mm1 = mm0;
    unsigned sa;
    int oth, mine;
    f32x16 zero;
#pragma unroll
    for (int i = 0; i < 16; ++i) zero[i] = 0.f;
    auto rd_frags = [&](const int slot) {
#pragma unroll
      for (int s = 0; s < 4; ++s) xf[s] = *(LDS bf16x8*)(lds + xrd[s] + slot * SLOT);
      ef = *(LDS bf16x8*)(lds + erd + slot * SLOT);
      sr0 = *(LDS bf16x8*)(lds + srow0); sr1 = *(LDS bf16x8*)(lds + srow1);
    };
    asm volatile("" ::: "memory");
    {
      const u32x4 A1 = *(LDS u32x4*)(lds + aoff + 128); Bq[1] = *(LDS unsigned*)(lds + boff + 128);
      dma_tile(1, 1, A1, Bq[1]);
      wait_dma(1);
      sa = (Bq[0] >> 26) ^ skey;
      *(LDS unsigned short*)(lds + sa) = 0x3F80;
      asm volatile("" ::: "memory");
      rd_frags(0);
      A4 = *(LDS u32x4*)(lds + aoff + 256); Bq[0] = *(LDS unsigned*)(lds + boff + 256);
      dma_tile(0, 2, A4, Bq[0]);
      a0 = MFMA(sr0, P2[0][0], zero); a0 = MFMA(sr1, P2[1][0], a0);
#pragma unroll
      for (int s = 0; s < 4; ++s) a0 = MFMA(xf[s], W1[s][0], a0);
      a0 = MFMA(ef, W3[0], a0);
    }
#define S0_ __builtin_bit_cast(bf16x8, __builtin_shufflevector(q00, q01, 0, 1, 2, 3, 4, 5, 6, 7))
#define S1_ __builtin_bit_cast(bf16x8, __builtin_shufflevector(q10, q11, 0, 1, 2, 3, 4, 5, 6, 7))
#define BF(v) __builtin_bit_cast(bf16x8, v)
#define PIN(lo, hi) _Pragma("unroll") for (int i = lo; i < hi; i += 4) asm volatile("" : "+v"(u[i]), "+v"(u[i + 1]), "+v"(u[i + 2]), "+v"(u[i + 3])); SB();
#define EXPS(lo, hi) _Pragma("unroll") for (int i = lo; i < hi; ++i) u[i] = __builtin_amdgcn_exp2f(c[i]); PIN(lo, hi)
#define ADDS(lo, hi) _Pragma("unroll") for (int i = lo; i < hi; ++i) u[i] = 1.0f + u[i]; PIN(lo, hi)
#define RCPS(lo, hi) _Pragma("unroll") for (int i = lo; i < hi; ++i) u[i] = __builtin_amdgcn_rcpf(u[i]); PIN(lo, hi)
#define FINS(lo, hi) _Pragma("unroll") for (int i = lo; i < hi; ++i) u[i] *= c[i]; PIN(lo, hi)
    auto phase = [&](const int T, const int par, const int t, f32x16& c, f32x16& nx, f32x16& Yc, f32x16& Yp) {
      float u[16];
      SB();
      Yp = MFMA(BF(mm1), S1_, Yp); SB();
      if (T == 0) {
        q00 = __builtin_amdgcn_ds_read_tr16_b64_v4i16((LDS s16x4*)(lds + str0));
        q01 = __builtin_amdgcn_ds_read_tr16_b64_v4i16((LDS s16x4*)(lds + str1));
        q10 = __builtin_amdgcn_ds_read_tr16_b64_v4i16((LDS s16x4*)(lds + str0 + 1024));
        q11 = __builtin_amdgcn_ds_read_tr16_b64_v4i16((LDS s16x4*)(lds + str1 + 1024));
        asm volatile("" ::: "memory");
        *(LDS unsigned short*)(lds + sa) = 0;
        sa = (Bq[par ^ 1] >> 26) ^ skey;
        SB();
      } else {
        dma_tile(par ^ 1, t + 3, A4, Bq[par ^ 1]);
        SB();
      }
      const int U = T ^ 1;
      nx = MFMA(sr0, P2[0][U], zero); SB();
      EXPS(0, 4)
      nx = MFMA(sr1, P2[1][U], nx); SB();
      EXPS(4, 8)
      nx = MFMA(xf[0], W1[0][U], nx); SB();
      EXPS(8, 12)
      nx = MFMA(xf[1], W1[1][U], nx); SB();
      EXPS(12, 16)
      nx = MFMA(xf[2], W1[2][U], nx); SB();
      ADDS(0, 16)
      nx = MFMA(xf[3], W1[3][U], nx); SB();
      RCPS(0, 4)
      nx = MFMA(ef, W3[U], nx); SB();
      if (T == 0) {
        wait_dma(t + 2);
        *(LDS unsigned short*)(lds + sa) = 0x3F80;
        asm volatile("" ::: "memory");
        rd_frags(par ^ 1);
        A4 = *(LDS u32x4*)(lds + aoff + (t + 3) * 128);
        Bq[par ^ 1] = *(LDS unsigned*)(lds + boff + (t + 3) * 128);
        const int left = n - wb - 32 * t;
        *(LDS int*)(lds + pa) = left; *(LDS int*)(lds + pa + 2048) = left;
        oth = *(LDS int*)(lds + pa + 1024);
        SB();
      }
      if (T == 1) {
        const int o = __builtin_amdgcn_readfirstlane(oth), left = __builtin_amdgcn_readfirstlane(n - wb - 32 * t);
        asm volatile("s_cmp_ge_i32 %0, %1\n\ts_cbranch_scc1 1f\n\ts_setprio 0\n\ts_branch 2f\n\t1:\n\ts_setprio 1\n\t2:" ::"s"(left), "s"(o) : "scc");
        SB();
      }
      RCPS(4, 16)
      FINS(0, 16)
      mm0 = u32x4{pk(u[0], u[1]), pk(u[2], u[3]), pk(u[4], u[5]), pk(u[6], u[7])}; SB();
      mm1 = u32x4{pk(u[8], u[9]), pk(u[10], u[11]), pk(u[12], u[13]), pk(u[14], u[15])}; SB();
      Yc = MFMA(BF(mm0), S0_, Yc); SB();
    };
    for (int t = 0; t < nt; t += 2) {
      phase(0, 0, t, a0, a1, Y0, Y1);
      phase(1, 0, t, a1, a0, Y1, Y0);
      if (t + 1 >= nt) break;
      phase(0, 1, t + 1, a0, a1, Y0, Y1);
      phase(1, 1, t + 1, a1, a0, Y1, Y0);
    }
    Y1 = MFMA(BF(mm1), S1_, Y1);
    *(LDS unsigned short*)(lds + sa) = 0;
    wb += CAP;
    if (wb >= n) break;
    *(LDS u32x4*)(lds + lb + (tid & 63) * 16) = u32x4{512, 512, 512, 512};
    *(LDS u32x4*)(lds + lb + 1024 + (tid & 63) * 16) = u32x4{512, 512, 512, 512};
    asm volatile("" ::: "memory");
    window_scan();
    window_gather(true);
    window_lists();
  }

  STAMP(5); STAMPC(1, __builtin_amdgcn_s_memtime());
#pragma unroll
  for (int i = 0; i < 16; ++i) { Y0[i] *= -0.6931471805599453f; Y1[i] *= -0.6931471805599453f; }
  f32x16 o0, o1;
#pragma unroll
  for (int i = 0; i < 16; ++i) { o0[i] = 0.f; o1[i] = 0.f; }
  auto ufrag = [&](int f) { return *(LDS bf16x8*)(lds + L_WU + f * 1024 + l * 16); };
#pragma unroll
  for (int s = 0; s < 4; ++s) { o0 = MFMA(ufrag(2 * s), XO[s], o0); o1 = MFMA(ufrag(2 * s + 1), XO[s], o1); }
#pragma unroll
  for (int s = 0; s < 2; ++s) {
    const bf16x8 z0 = __builtin_bit_cast(bf16x8, u32x4{pk(Y0[8 * s], Y0[8 * s + 1]), pk(Y0[8 * s + 2], Y0[8 * s + 3]), pk(Y0[8 * s + 4], Y0[8 * s + 5]), pk(Y0[8 * s + 6], Y0[8 * s + 7])});
    const bf16x8 z1 = __builtin_bit_cast(bf16x8, u32x4{pk(Y1[8 * s], Y1[8 * s + 1]), pk(Y1[8 * s + 2], Y1[8 * s + 3]), pk(Y1[8 * s + 4], Y1[8 * s + 5]), pk(Y1[8 * s + 6], Y1[8 * s + 7])});
    o0 = MFMA(ufrag(8 + 2 * s), z0, o0); o1 = MFMA(ufrag(9 + 2 * s), z0, o1);
    o0 = MFMA(ufrag(12 + 2 * s), z1, o0); o1 = MFMA(ufrag(13 + 2 * s), z1, o1);
  }
#pragma unroll
  for (int q = 0; q < 4; ++q) {
    const u32x2 v0 = {pk(silu1(o0[4 * q]), silu1(o0[4 * q + 1])), pk(silu1(o0[4 * q + 2]), silu1(o0[4 * q + 3]))};
    const u32x2 v1 = {pk(silu1(o1[4 * q]), silu1(o1[4 * q + 1])), pk(silu1(o1[4 * q + 2]), silu1(o1[4 * q + 3]))};
    *(LDS u32x2*)(lds + stg + r * 128 + ((q ^ (r & 7)) << 4) + h * 8) = v0;
    *(LDS u32x2*)(lds + stg + r * 128 + (((4 + q) ^ (r & 7)) << 4) + h * 8) = v1;
  }
  asm volatile("" ::: "memory");
#pragma unroll
  for (int it = 0; it < 4; ++it) {
    const int m = l + 64 * it, row = m >> 3, p = m & 7;
    if (m < ncnt * 8) {
      const u32x4 v = *(LDS u32x4*)(lds + stg + row * 128 + ((p ^ (row & 7)) << 4));
      *(u32x4*)(out + (size_t)nbase * 128 + m * 16) = v;
    }
  }
  STAMP(6);
  asm volatile(".Lkm_code_end:");
}

static inline void launch_part(void* const* d_in, void* d_ws, hipStream_t stream) {
  k_part<<<NC + 5, 512, 0, stream>>>((const i32x4*)d_in[3], (const i32x4*)d_in[2], (const u32x4*)d_in[0], (const uint16_t*)d_in[4], (const uint16_t*)d_in[5], (char*)d_ws);
}
static inline void launch_main(void* const* d_in, void* d_out, void* d_ws, hipStream_t stream) {
  k_main<<<NB, 512, 0, stream>>>((const char*)d_in[0], (const char*)d_in[1], (const char*)d_ws, (char*)d_out);
}

extern "C" void kernel_launch(void* const* d_in, const int* in_sizes, int n_in, void* d_out, int out_size, void* d_ws, size_t ws_size,
                              hipStream_t stream) {
  launch_part(d_in, d_ws, stream);
  launch_main(d_in, d_out, d_ws, stream);
}
```

```cpp
#include <hip/hip_runtime.h>
#include <stdint.h>

#define LDS __attribute__((address_space(3)))
#define DI __device__ __forceinline__
typedef float f32x16 __attribute__((ext_vector_type(16)));
typedef float f32x2 __attribute__((ext_vector_type(2)));
typedef unsigned u32x4 __attribute__((ext_vector_type(4)));
typedef unsigned u32x2 __attribute__((ext_vector_type(2)));
typedef int i32x4 __attribute__((ext_vector_type(4)));
typedef __bf16 bf16x8 __attribute__((ext_vector_type(8)));
typedef __bf16 bf16x2 __attribute__((ext_vector_type(2)));
typedef short s16x4 __attribute__((ext_vector_type(4)));

constexpr int NN = 50000;
constexpr int NE = 800000;
constexpr int GP = 25;
constexpr int CE = 3200;
constexpr int NC = 250;
constexpr int NB = 256;
constexpr int TROW = 4096;
constexpr unsigned HALF = 25000;
constexpr int CAP = 512;
constexpr unsigned WS_META = 25600000u, WS_OFF = 28800000u, WS_WF = 29824000u;

#define MFMA(a, b, c) __builtin_amdgcn_mfma_f32_32x32x16_bf16((a), (b), (c), 0, 0, 0)
#define BAR() asm volatile("s_waitcnt lgkmcnt(0)\n\ts_barrier" ::: "memory")
#define SB() __builtin_amdgcn_sched_barrier(0)
#define EPOL "nt"

DI unsigned pk(float a, float b) { f32x2 v = {a, b}; bf16x2 r = __builtin_convertvector(v, bf16x2); return __builtin_bit_cast(unsigned, r); }
DI unsigned wave_incl_scan(unsigned x) {
  x += __builtin_amdgcn_update_dpp(0u, x, 0x111, 0xf, 0xf, false);
  x += __builtin_amdgcn_update_dpp(0u, x, 0x112, 0xf, 0xf, false);
  x += __builtin_amdgcn_update_dpp(0u, x, 0x114, 0xf, 0xf, false);
  x += __builtin_amdgcn_update_dpp(0u, x, 0x118, 0xf, 0xf, false);
  x += __builtin_amdgcn_update_dpp(0u, x, 0x142, 0xa, 0xf, false);
  x += __builtin_amdgcn_update_dpp(0u, x, 0x143, 0xc, 0xf, false);
  return x;
}

__global__ __launch_bounds__(512) void k_part(const i32x4* __restrict__ dst4, const i32x4* __restrict__ src4,
                                              const uint16_t* __restrict__ wm, const uint16_t* __restrict__ wu, char* __restrict__ ws) {
  __shared__ __attribute__((aligned(16))) unsigned cnt[2048];
  __shared__ __attribute__((aligned(16))) unsigned metaL[CE];
  __shared__ unsigned wsum[8];
  const int tid = threadIdx.x, c = blockIdx.x;
  if (c >= NC) {
    const int it = (c - NC) * 512 + tid;
    if (it < 34 * 64) {
      const int f = it >> 6, l = it & 63, col = l & 31, h = l >> 5;
      unsigned short v[8];
#pragma unroll
      for (int j = 0; j < 8; ++j) {
        if (f < 18) {
          const int t = f & 1, s = (f >> 1) & 3;
          const int row = (f < 8 ? 0 : (f < 16 ? 64 : 128)) + (f < 16 ? 16 * s : 0) + 8 * h + j;
          v[j] = wm[row * 64 + 32 * t + col];
        } else {
          const int ks = (f - 18) >> 1, u = f & 1;
          const int row = ks < 4 ? 16 * ks + 8 * h + j : 64 + 32 * ((ks - 4) >> 1) + 16 * (ks & 1) + 8 * (j >> 2) + 4 * h + (j & 3);
          v[j] = wu[row * 64 + 32 * u + col];
        }
      }
      u32x4 o = {v[0] | ((unsigned)v[1] << 16), v[2] | ((unsigned)v[3] << 16), v[4] | ((unsigned)v[5] << 16), v[6] | ((unsigned)v[7] << 16)};
      if (f < 18) {
#pragma unroll
        for (int q = 0; q < 4; ++q)
          o[q] = pk(__builtin_bit_cast(float, o[q] << 16) * -1.4426950408889634f, __builtin_bit_cast(float, o[q] & 0xFFFF0000u) * -1.4426950408889634f);
      }
      *(u32x4*)(ws + WS_WF + it * 16) = o;
    }
    return;
  }
  constexpr int TA = CE / 8;
  i32x4 d[2], s[2];
  const int ta = tid < TA ? tid : TA - 1;
#pragma unroll
  for (int i = 0; i < 2; ++i) { d[i] = dst4[c * (CE / 4) + ta + i * TA]; s[i] = src4[c * (CE / 4) + ta + i * TA]; }
  unsigned cw[2] = {0, 0};
  if (c < 8 && __builtin_amdgcn_readfirstlane(tid >> 6) == 7) {
    unsigned long long pc; int off; unsigned len;
    asm volatile("s_getpc_b64 %0\n.Lkp_code_here:\n\ts_mov_b32 %1, .Lkm_code_begin-.Lkp_code_here\n\ts_mov_b32 %2, .Lkm_code_end-.Lkm_code_begin" : "=s"(pc), "=s"(off), "=s"(len));
    typedef __attribute__((address_space(1))) const unsigned* gptr;
    const unsigned long long base = (pc + off) & ~255ull;
#pragma unroll
    for (int i = 0; i < 2; ++i) { const unsigned o = (i * 64 + (tid & 63)) * 128; cw[i] = *(gptr)(base + (o < len - 4 ? o : len - 4)); }
  }
  *(u32x4*)&cnt[4 * tid] = u32x4{0, 0, 0, 0};
  BAR();
  unsigned g[8], rk[8];
  if (tid < TA) {
#pragma unroll
    for (int j = 0; j < 8; ++j) { g[j] = ((unsigned)d[j >> 2][j & 3] * 128u + 127u) / 3125u; rk[j] = __hip_atomic_fetch_add(&cnt[g[j]], 1u, __ATOMIC_RELAXED, __HIP_MEMORY_SCOPE_WORKGROUP); }
  }
  BAR();
  const u32x4 cc = *(const u32x4*)&cnt[4 * tid];
  const unsigned tot = cc[0] + cc[1] + cc[2] + cc[3];
  const unsigned incl = wave_incl_scan(tot);
  if ((tid & 63) == 63) wsum[tid >> 6] = incl;
  BAR();
  unsigned ws8 = wsum[tid & 7];
  ws8 += __builtin_amdgcn_update_dpp(0u, ws8, 0x111, 0xf, 0xf, false);
  ws8 += __builtin_amdgcn_update_dpp(0u, ws8, 0x112, 0xf, 0xf, false);
  ws8 += __builtin_amdgcn_update_dpp(0u, ws8, 0x114, 0xf, 0xf, false);
  const int w = __builtin_amdgcn_readfirstlane(tid >> 6);
  const unsigned wb = w ? __builtin_amdgcn_readlane(ws8, w - 1) : 0u;
  const unsigned o0 = wb + incl - tot, o1 = o0 + cc[0], o2 = o1 + cc[1], o3 = o2 + cc[2];
  *(u32x4*)&cnt[4 * tid] = u32x4{o0, o1, o2, o3};
  *(u32x2*)(ws + WS_OFF + c * TROW + tid * 8) = u32x2{o0 | (o1 << 16), o2 | (o3 << 16)};
  BAR();
  if (tid < TA) {
#pragma unroll
    for (int j = 0; j < 8; ++j) {
      const unsigned dv = d[j >> 2][j & 3], sv = s[j >> 2][j & 3];
      metaL[cnt[g[j]] + rk[j]] = sv * (GP * CE) + (dv - (__umul24(g[j], 3125u) >> 7)) * CE + (4 * tid + (j & 3) + (j >> 2) * (4 * TA));
    }
  }
  BAR();
  if (tid < TA) {
#pragma unroll
    for (int i = 0; i < 2; ++i) *(u32x4*)(ws + WS_META + c * (CE * 4) + (tid + i * TA) * 16) = *(const u32x4*)&metaL[4 * (tid + i * TA)];
  }
  asm volatile("" ::"v"(cw[0]), "v"(cw[1]));
}

constexpr unsigned L_STG = 0;
constexpr unsigned SLOT = 40960;
constexpr unsigned L_S = 81920;
constexpr unsigned L_LIST = 98304;
constexpr unsigned L_WU = 131072;
constexpr unsigned L_T1 = 147456;
constexpr unsigned L_T2 = 151552;
constexpr unsigned L_PRG = 155648;
constexpr unsigned L_END = 158720;
constexpr unsigned L_M = 0;
constexpr unsigned L_OWN = 40960;
constexpr unsigned L_WIMG = 73728;
constexpr unsigned L_PF = 92160;
constexpr int PFW = 1;

DI void dma16(unsigned m0, unsigned voff, __amdgpu_buffer_rsrc_t srd, unsigned soff) {
  asm volatile("s_mov_b32 m0, %0\n\ts_nop 0\n\tbuffer_load_dwordx4 %1, %2, %3 offen lds" ::"s"(m0), "v"(voff), "s"(srd), "s"(soff) : "m0", "memory");
}
DI unsigned umax(unsigned a, unsigned b) { return a > b ? a : b; }
DI unsigned wave_excl_max(unsigned x) {
  x = umax(x, __builtin_amdgcn_update_dpp(0u, x, 0x111, 0xf, 0xf, false));
  x = umax(x, __builtin_amdgcn_update_dpp(0u, x, 0x112, 0xf, 0xf, false));
  x = umax(x, __builtin_amdgcn_update_dpp(0u, x, 0x114, 0xf, 0xf, false));
  x = umax(x, __builtin_amdgcn_update_dpp(0u, x, 0x118, 0xf, 0xf, false));
  x = umax(x, __builtin_amdgcn_update_dpp(0u, x, 0x142, 0xa, 0xf, false));
  x = umax(x, __builtin_amdgcn_update_dpp(0u, x, 0x143, 0xc, 0xf, false));
  return __builtin_amdgcn_update_dpp(0u, x, 0x138, 0xf, 0xf, false);
}
DI float silu1(float v) { return v * __builtin_amdgcn_rcpf(1.0f + __builtin_amdgcn_exp2f(v * -1.4426950408889634f)); }

__global__ __launch_bounds__(512, 2) void k_main(const char* __restrict__ x, const char* __restrict__ ea, const char* __restrict__ ws, char* __restrict__ out) {
  __shared__ __attribute__((aligned(1024))) char lds_[L_END];
  asm volatile(".Lkm_code_begin:");
  LDS char* lds = (LDS char*)lds_;
  const unsigned ldsb = (unsigned)(size_t)lds;
  const int tid = threadIdx.x, l = tid & 63, w = __builtin_amdgcn_readfirstlane(tid >> 6);
  const int xcd = blockIdx.x & 7, b = xcd * 32 + (blockIdx.x >> 3);
  const int g = 8 * b + w, nbase = (g * 3125) >> 7, ncnt = (((g + 1) * 3125) >> 7) - nbase;
  const int r = l & 31, h = l >> 5;
  const auto sx = __builtin_amdgcn_make_buffer_rsrc((void*)x, 0, NN * 128, 0x00020000);
  const auto se = __builtin_amdgcn_make_buffer_rsrc((void*)ea, 0, NE * 32, 0x00020000);
  const auto sm = __builtin_amdgcn_make_buffer_rsrc((void*)(ws + WS_META), 0, NE * 4, 0x00020000);
  const auto sw = __builtin_amdgcn_make_buffer_rsrc((void*)(ws + WS_WF), 0, 34 * 1024, 0x00020000);

  const auto st = __builtin_amdgcn_make_buffer_rsrc((void*)(ws + WS_OFF), 0, NC * TROW, 0x00020000);

  const unsigned stg = L_STG + w * 5120;
  asm volatile("s_nop 4" ::: "memory");
  dma16(ldsb + L_T1 + w * 1024, (tid & 255) * TROW + b * 16 + (w >> 2) * 16, st, 0);
  const unsigned bx = blockIdx.x;
  {
    unsigned long long pc; unsigned len;
    asm volatile("s_getpc_b64 %0\n.Lkm_code_here:\n\ts_mov_b32 %1, .Lkm_code_end-.Lkm_code_here" : "=s"(pc), "=s"(len));
    const auto sc = __builtin_amdgcn_make_buffer_rsrc((void*)pc, 0, len, 0x00020000);
    const unsigned vo = l < 16 ? (w * 16 + l) * 128 : 0x7FFFFFF0u;
    asm volatile("s_cmp_lt_u32 %3, %4\n\ts_cbranch_scc0 1f\n\ts_nop 4\n\ts_mov_b32 m0, %0\n\ts_nop 0\n\tbuffer_load_dword %1, %2, 0 offen lds\n\t1:"
                 ::"s"(ldsb + L_PF + w * 256), "v"(vo), "s"(sc), "s"(bx), "n"(8 * PFW) : "m0", "scc", "memory");
    const auto sp = __builtin_amdgcn_make_buffer_rsrc((void*)(w < 6 ? ea : x), 0, w < 6 ? NE * 32 : NN * 128, 0x00020000);
    const unsigned vt = w < 6 ? __umul24(tid, 66688u) : __umul24(tid - 384, 50000u);
    asm volatile("s_cmp_lt_u32 %3, %4\n\ts_cbranch_scc0 1f\n\ts_nop 4\n\ts_mov_b32 m0, %0\n\ts_nop 0\n\tbuffer_load_dword %1, %2, 0 offen lds\n\t1:"
                 ::"s"(ldsb + L_PF + w * 256), "v"(vt), "s"(sp), "s"(bx), "n"(8 * PFW) : "m0", "scc", "memory");
  }
  const unsigned sreg = L_S + w * 2048;
  const unsigned la = L_LIST + w * 4096, lb = la + 2048;
  *(LDS u32x4*)(lds + lb + l * 16) = u32x4{512, 512, 512, 512};
  *(LDS u32x4*)(lds + lb + 1024 + l * 16) = u32x4{512, 512, 512, 512};
  f32x16 Y0, Y1;
#pragma unroll
  for (int i = 0; i < 16; ++i) { Y0[i] = 0.f; Y1[i] = 0.f; }

  unsigned xch[4];
#pragma unroll
  for (int s = 0; s < 4; ++s) xch[s] = ((l & 7) ^ (((8 * s + (l >> 3)) >> 1) & 7)) << 4;
  unsigned ech = ((l & 1) ^ ((l >> 4) & 1)) << 4;
  unsigned skey = sreg + (l >> 1) * 64 + (((l >> 3) & 3) << 4);
  unsigned aoff = la + (l >> 3) * 16;
  unsigned boff = lb + (4 * ((l >> 1) & 7) + (l >> 4)) * 4;
  unsigned xrd[4];
#pragma unroll
  for (int s = 0; s < 4; ++s) xrd[s] = stg + r * 128 + (((2 * s + h) ^ ((r >> 1) & 7)) << 4);
  unsigned erd = stg + 4096 + r * 32 + ((h ^ ((r >> 3) & 1)) << 4);
  unsigned srow0 = sreg + r * 64 + ((h ^ ((r >> 2) & 3)) << 4), srow1 = srow0 ^ 32;
  unsigned str0 = sreg + (4 * h + ((l & 15) >> 2)) * 64 + (((2 * ((l >> 4) & 1) + ((l & 3) >> 1)) ^ h) << 4) + (l & 1) * 8;
  unsigned str1 = (str0 ^ 32) + 512;

  asm volatile("" : "+v"(xch[0]), "+v"(xch[1]), "+v"(ech), "+v"(skey), "+v"(aoff), "+v"(boff), "+v"(xrd[0]), "+v"(xrd[1]), "+v"(xrd[2]), "+v"(xrd[3]));
  asm volatile("" : "+v"(erd), "+v"(srow0), "+v"(srow1), "+v"(str0), "+v"(str1), "+v"(Y0), "+v"(Y1));
  xch[2] = xch[0]; xch[3] = xch[1];
  asm volatile("s_cmp_lt_u32 %0, %1\n\ts_cbranch_scc0 1f\n\ts_waitcnt vmcnt(2)\n\ts_branch 2f\n\t1:\n\ts_waitcnt vmcnt(0)\n\t2:" ::"s"(bx), "n"(8 * PFW) : "scc", "memory");
  if (b == NB - 1 && w >= 4) *(LDS unsigned short*)(lds + L_T2 + (tid & 255) * 16) = (tid & 255) < NC ? CE : 0;
  BAR();
  {
    const unsigned c0 = 32 * w + (l >> 3);
    unsigned s0[4], s1[4], vo[4];
#pragma unroll
    for (int i = 0; i < 4; ++i) { s0[i] = *(LDS unsigned short*)(lds + L_T1 + c0 * 16 + i * 128); s1[i] = *(LDS unsigned short*)(lds + L_T2 + c0 * 16 + i * 128); }
#pragma unroll
    for (int i = 0; i < 4; ++i) {
      const unsigned o = (s0[i] & ~3u) + 4 * (l & 7);
      vo[i] = o < s1[i] ? (__umul24(c0 + 8 * i, CE) + o) * 4 : 0x7FFFFFF0u;
    }
#pragma unroll
    for (int i = 0; i < 4; ++i) dma16(ldsb + L_M + (32 * w + 8 * i) * 128, vo[i], sm, 0);
  }

  int wb = 0, n, nw;
  unsigned cj[8], mt[8];
  auto window_scan = [&]() {
    int l = tid & 63;
    asm volatile("" : "+v"(l));
    unsigned s0[4], o0[4], o1[4], cn[4], rb[4], ix[4];
    const unsigned ta = L_T1 + l * 16, tb = ta + w * 2, tc = l * 16 + (w < 7 ? L_T1 + w * 2 + 2 : L_T2);
#pragma unroll
    for (int k = 0; k < 4; ++k) {
      s0[k] = *(LDS unsigned short*)(lds + ta + k * 1024);
      o0[k] = *(LDS unsigned short*)(lds + tb + k * 1024);
      o1[k] = *(LDS unsigned short*)(lds + tc + k * 1024);
    }
    unsigned tot = 0;
#pragma unroll
    for (int k = 0; k < 4; ++k) {
      cn[k] = o1[k] - o0[k];
      if (k == 3) cn[k] = l < NC - 192 ? cn[k] : 0u;
      rb[k] = (l << 13) + o0[k] - (s0[k] & ~3u) + ((k << 19) + 512 + (1 << 21));
      ix[k] = tot;
      tot += cn[k];
    }
    const unsigned incl = wave_incl_scan(tot);
    n = __builtin_amdgcn_readlane(incl, 63);
    nw = n - wb < CAP ? n - wb : CAP;
    const int base = (int)(incl - tot) - wb;
#pragma unroll
    for (int k = 0; k < 4; ++k) {
      const int lo = base + (int)ix[k];
      const int pos = lo > 0 ? lo : 0;
      const bool on = pos < lo + (int)cn[k] && pos < CAP;
      *(LDS unsigned*)(lds + (on ? lb + pos * 4 : la + l * 4)) = (pos << 21) + (rb[k] - lo);
    }
    asm volatile("" ::: "memory");
    const u32x4 m0 = *(LDS u32x4*)(lds + lb + l * 32), m1 = *(LDS u32x4*)(lds + lb + l * 32 + 16);
    unsigned run[8];
    run[0] = m0[0];
#pragma unroll
    for (int j = 1; j < 8; ++j) run[j] = umax(run[j - 1], j < 4 ? m0[j] : m1[j - 4]);
    const unsigned ex = wave_excl_max(run[7]);
    const int last = (nw > 0 ? nw - 1 : 0) - 512, i0 = 8 * l - 512;
#pragma unroll
    for (int j = 0; j < 8; ++j) {
      const int im = i0 < last - j ? i0 : last - j;
      cj[j] = (umax(run[j], ex) & 0x1FFFFFu) + im + j;
    }
  };
  auto window_gather = [&](bool all) {
#pragma unroll
    for (int j = 0; j < 8; ++j) {
      const unsigned c = cj[j] >> 13, q = cj[j] & 0x1FFFu;
      const unsigned s0 = *(LDS unsigned short*)(lds + L_T1 + c * 16) & ~3u;
      const unsigned v = __builtin_amdgcn_raw_buffer_load_b32(sm, (__umul24(c, CE) + s0 + q) * 4, 0, 0);
      mt[j] = (all || q >= 32) ? v : mt[j];
    }
  };
  window_scan();
  unsigned pa = L_PRG + w * 256 + l * 4;
  asm volatile("" : "+v"(pa));
  *(LDS int*)(lds + pa) = n; *(LDS int*)(lds + pa + 2048) = n;
#pragma unroll
  for (int i = 0; i < 5; ++i) {
    const int f = w + 8 * i;
    const unsigned lo = (i < 4 ? L_WIMG : L_WU - 18 * 1024) + f * 1024, hi = i < 4 ? L_WU - 18 * 1024 + f * 1024 : L_PF + 2048;
    dma16(ldsb + (i < 2 ? lo : (i == 3 ? hi : (w < 2 ? lo : hi))), l * 16, sw, (i < 4 ? f : (w < 2 ? f : 33)) * 1024);
  }
  const unsigned own = L_OWN + w * 4096;
#pragma unroll
  for (int s = 0; s < 4; ++s) {
    const int R = 8 * s + (l >> 3);
    int node = nbase + R; node = node < NN ? node : NN - 1;
    dma16(ldsb + own + s * 1024, node * 128 + (((l & 7) ^ ((R >> 1) & 7)) << 4), sx, 0);
  }
  asm volatile("s_waitcnt vmcnt(9)" ::: "memory");
  BAR();
  {
    unsigned acc = 0;
#pragma unroll
    for (int j = 0; j < 8; ++j) {
      acc |= cj[j];
      mt[j] = *(LDS unsigned*)(lds + L_M + (((cj[j] >> 13) << 5) + (cj[j] & 31)) * 4);
    }
    const bool over = (acc & 0x1FE0u) != 0;
    BAR();
    if (__any(over)) window_gather(false);
  }
  int nt;
  u32x4 A4;
  unsigned Bq[2];
  auto dma_tile = [&](const int slot, const int tt, const u32x4 A, const unsigned B) {
    const unsigned v0 = A[0] | xch[0], v1 = A[1] | xch[1], v2 = A[2] | xch[2], v3 = A[3] | xch[3], v4 = (B & 0x07FFFFFFu) | ech;
    asm volatile("s_waitcnt lgkmcnt(0)\n\t"
                 "s_cmp_lt_i32 %[t], %[nt]\n\ts_cbranch_scc0 1f\n\t"
                 "s_mov_b32 m0, %[m]\n\ts_nop 0\n\tbuffer_load_dwordx4 %[v0], %[sx], 0 offen lds\n\t"
                 "s_add_u32 m0, %[m], 0x400\n\ts_nop 0\n\tbuffer_load_dwordx4 %[v1], %[sx], 0 offen lds\n\t"
                 "s_add_u32 m0, %[m], 0x800\n\ts_nop 0\n\tbuffer_load_dwordx4 %[v2], %[sx], 0 offen lds\n\t"
                 "s_add_u32 m0, %[m], 0xc00\n\ts_nop 0\n\tbuffer_load_dwordx4 %[v3], %[sx], 0 offen lds\n\t"
                 "s_add_u32 m0, %[m], 0x1000\n\ts_nop 0\n\tbuffer_load_dwordx4 %[v4], %[se], 0 offen " EPOL " lds\n\t"
                 "1:"
                 ::[t] "s"(tt), [nt] "s"(nt), [m] "s"(ldsb + stg + slot * SLOT), [v0] "v"(v0), [v1] "v"(v1), [v2] "v"(v2), [v3] "v"(v3), [v4] "v"(v4), [sx] "s"(sx), [se] "s"(se)
                 : "m0", "scc", "memory");
  };
  auto wait_dma = [&](const int tt) {
    asm volatile("s_cmp_lt_i32 %0, %1\n\ts_cbranch_scc1 1f\n\ts_waitcnt vmcnt(0)\n\t1:\n\ts_waitcnt vmcnt(5)" ::"s"(tt), "s"(nt) : "scc", "memory");
  };
  auto window_lists = [&]() {
    nt = (nw + 31) >> 5;
    {
      int l = tid & 63;
      asm volatile("" : "+v"(l));
      unsigned av[8], bv[8];
#pragma unroll
      for (int j = 0; j < 8; ++j) {
        const unsigned t = mt[j] >> 7, sn = __umulhi(t, 54975582u) >> 3;
        const unsigned u = (unsigned)__mul24((int)sn, -625) + t;
        const unsigned dl = __umul24(u, 41u) >> 10;
        const unsigned eh = (unsigned)__mul24((int)dl, -25) + __umul24(cj[j] >> 13, 25u) + u;
        av[j] = sn << 7;
        bv[j] = (eh << 12) | ((mt[j] << 5) & 0xFE0u) | ((8 * l + j < nw ? dl : 31u) << 27);
      }
      unsigned cl = 0;
      bool lo[8];
#pragma unroll
      for (int j = 0; j < 8; ++j) { lo[j] = (av[j] < HALF * 128u) != (b >= NB / 2) && 8 * l + j < nw; cl += lo[j]; }
      const unsigned incl = wave_incl_scan(cl);
      unsigned pl = incl - cl;
      const unsigned hb = (unsigned)__builtin_amdgcn_readlane(incl, 63) + 8 * l;
#pragma unroll
      for (int j = 0; j < 8; ++j) {
        const unsigned pos = lo[j] ? pl : hb + j - pl;
        pl += lo[j];
        *(LDS unsigned*)(lds + la + pos * 4) = av[j];
        *(LDS unsigned*)(lds + la + pos * 4 + 2048) = bv[j];
      }
      asm volatile("" ::: "memory");
    }
    A4 = *(LDS u32x4*)(lds + aoff); Bq[0] = *(LDS unsigned*)(lds + boff);
    dma_tile(0, 0, A4, Bq[0]);
  };
  window_lists();
  wait_dma(0);
  BAR();
  bf16x8 W1[4][2], W3[2], P2[2][2], XO[4];
  {
    auto wfrag = [&](int f) { return *(LDS bf16x8*)(lds + L_WIMG + f * 1024 + l * 16); };
#pragma unroll
    for (int s = 0; s < 4; ++s) { W1[s][0] = wfrag(2 * s); W1[s][1] = wfrag(2 * s + 1); }
    W3[0] = wfrag(16); W3[1] = wfrag(17);
    const int rs = (r & 19) | ((r & 4) << 1) | ((r & 8) >> 1);
    f32x16 p0, p1;
#pragma unroll
    for (int i = 0; i < 16; ++i) { p0[i] = 0.f; p1[i] = 0.f; }
#pragma unroll
    for (int s = 0; s < 4; ++s) {
      const bf16x8 a = *(LDS bf16x8*)(lds + own + rs * 128 + (((2 * s + h) ^ ((rs >> 1) & 7)) << 4));
      p0 = MFMA(a, wfrag(8 + 2 * s), p0);
      p1 = MFMA(a, wfrag(9 + 2 * s), p1);
      XO[s] = *(LDS bf16x8*)(lds + own + r * 128 + (((2 * s + h) ^ ((r >> 1) & 7)) << 4));
    }
#pragma unroll
    for (int s = 0; s < 2; ++s) {
      P2[s][0] = __builtin_bit_cast(bf16x8, u32x4{pk(p0[8 * s], p0[8 * s + 1]), pk(p0[8 * s + 2], p0[8 * s + 3]), pk(p0[8 * s + 4], p0[8 * s + 5]), pk(p0[8 * s + 6], p0[8 * s + 7])});
      P2[s][1] = __builtin_bit_cast(bf16x8, u32x4{pk(p1[8 * s], p1[8 * s + 1]), pk(p1[8 * s + 2], p1[8 * s + 3]), pk(p1[8 * s + 4], p1[8 * s + 5]), pk(p1[8 * s + 6], p1[8 * s + 7])});
    }
  }
  BAR();
  *(LDS u32x4*)(lds + sreg + l * 16) = u32x4{0, 0, 0, 0};
  *(LDS u32x4*)(lds + sreg + 1024 + l * 16) = u32x4{0, 0, 0, 0};
  while (true) {
    f32x16 a0, a1;
    bf16x8 xf[4], ef, sr0, sr1;
    s16x4 q00 = {0, 0, 0, 0}, q01 = q00, q10 = q00, q11 = q00;
    u32x4 mm0 = {0, 0, 0, 0}, mm1 = mm0;
    unsigned sa;
    int oth, mine;
    f32x16 zero;
#pragma unroll
    for (int i = 0; i < 16; ++i) zero[i] = 0.f;
    auto rd_frags = [&](const int slot) {
#pragma unroll
      for (int s = 0; s < 4; ++s) xf[s] = *(LDS bf16x8*)(lds + xrd[s] + slot * SLOT);
      ef = *(LDS bf16x8*)(lds + erd + slot * SLOT);
      sr0 = *(LDS bf16x8*)(lds + srow0); sr1 = *(LDS bf16x8*)(lds + srow1);
    };
    asm volatile("" ::: "memory");
    {
      const u32x4 A1 = *(LDS u32x4*)(lds + aoff + 128); Bq[1] = *(LDS unsigned*)(lds + boff + 128);
      dma_tile(1, 1, A1, Bq[1]);
      wait_dma(1);
      sa = (Bq[0] >> 26) ^ skey;
      *(LDS unsigned short*)(lds + sa) = 0x3F80;
      asm volatile("" ::: "memory");
      rd_frags(0);
      A4 = *(LDS u32x4*)(lds + aoff + 256); Bq[0] = *(LDS unsigned*)(lds + boff + 256);
      dma_tile(0, 2, A4, Bq[0]);
      a0 = MFMA(sr0, P2[0][0], zero); a0 = MFMA(sr1, P2[1][0], a0);
#pragma unroll
      for (int s = 0; s < 4; ++s) a0 = MFMA(xf[s], W1[s][0], a0);
      a0 = MFMA(ef, W3[0], a0);
    }
#define S0_ __builtin_bit_cast(bf16x8, __builtin_shufflevector(q00, q01, 0, 1, 2, 3, 4, 5, 6, 7))
#define S1_ __builtin_bit_cast(bf16x8, __builtin_shufflevector(q10, q11, 0, 1, 2, 3, 4, 5, 6, 7))
#define BF(v) __builtin_bit_cast(bf16x8, v)
#define PIN(lo, hi) _Pragma("unroll") for (int i = lo; i < hi; i += 4) asm volatile("" : "+v"(u[i]), "+v"(u[i + 1]), "+v"(u[i + 2]), "+v"(u[i + 3])); SB();
#define EXPS(lo, hi) _Pragma("unroll") for (int i = lo; i < hi; ++i) u[i] = __builtin_amdgcn_exp2f(c[i]); PIN(lo, hi)
#define ADDS(lo, hi) _Pragma("unroll") for (int i = lo; i < hi; ++i) u[i] = 1.0f + u[i]; PIN(lo, hi)
#define RCPS(lo, hi) _Pragma("unroll") for (int i = lo; i < hi; ++i) u[i] = __builtin_amdgcn_rcpf(u[i]); PIN(lo, hi)
#define FINS(lo, hi) _Pragma("unroll") for (int i = lo; i < hi; ++i) u[i] *= c[i]; PIN(lo, hi)
    auto phase = [&](const int T, const int par, const int t, f32x16& c, f32x16& nx, f32x16& Yc, f32x16& Yp) {
      float u[16];
      SB();
      Yp = MFMA(BF(mm1), S1_, Yp); SB();
      if (T == 0) {
        q00 = __builtin_amdgcn_ds_read_tr16_b64_v4i16((LDS s16x4*)(lds + str0));
        q01 = __builtin_amdgcn_ds_read_tr16_b64_v4i16((LDS s16x4*)(lds + str1));
        q10 = __builtin_amdgcn_ds_read_tr16_b64_v4i16((LDS s16x4*)(lds + str0 + 1024));
        q11 = __builtin_amdgcn_ds_read_tr16_b64_v4i16((LDS s16x4*)(lds + str1 + 1024));
        asm volatile("" ::: "memory");
        *(LDS unsigned short*)(lds + sa) = 0;
        sa = (Bq[par ^ 1] >> 26) ^ skey;
        SB();
      } else {
        dma_tile(par ^ 1, t + 3, A4, Bq[par ^ 1]);
        SB();
      }
      const int U = T ^ 1;
      nx = MFMA(sr0, P2[0][U], zero); SB();
      EXPS(0, 4)
      nx = MFMA(sr1, P2[1][U], nx); SB();
      EXPS(4, 8)
      nx = MFMA(xf[0], W1[0][U], nx); SB();
      EXPS(8, 12)
      nx = MFMA(xf[1], W1[1][U], nx); SB();
      EXPS(12, 16)
      nx = MFMA(xf[2], W1[2][U], nx); SB();
      ADDS(0, 16)
      nx = MFMA(xf[3], W1[3][U], nx); SB();
      RCPS(0, 4)
      nx = MFMA(ef, W3[U], nx); SB();
      if (T == 0) {
        wait_dma(t + 2);
        *(LDS unsigned short*)(lds + sa) = 0x3F80;
        asm volatile("" ::: "memory");
        rd_frags(par ^ 1);
        A4 = *(LDS u32x4*)(lds + aoff + (t + 3) * 128);
        Bq[par ^ 1] = *(LDS unsigned*)(lds + boff + (t + 3) * 128);
        const int left = n - wb - 32 * t;
        *(LDS int*)(lds + pa) = left; *(LDS int*)(lds + pa + 2048) = left;
        oth = *(LDS int*)(lds + pa + 1024);
        SB();
      }
      if (T == 1) {
        const int o = __builtin_amdgcn_readfirstlane(oth), left = __builtin_amdgcn_readfirstlane(n - wb - 32 * t);
        asm volatile("s_cmp_ge_i32 %0, %1\n\ts_cbranch_scc1 1f\n\ts_setprio 0\n\ts_branch 2f\n\t1:\n\ts_setprio 1\n\t2:" ::"s"(left), "s"(o) : "scc");
        SB();
      }
      RCPS(4, 16)
      FINS(0, 16)
      mm0 = u32x4{pk(u[0], u[1]), pk(u[2], u[3]), pk(u[4], u[5]), pk(u[6], u[7])}; SB();
      mm1 = u32x4{pk(u[8], u[9]), pk(u[10], u[11]), pk(u[12], u[13]), pk(u[14], u[15])}; SB();
      Yc = MFMA(BF(mm0), S0_, Yc); SB();
    };
    for (int t = 0; t < nt; t += 2) {
      phase(0, 0, t, a0, a1, Y0, Y1);
      phase(1, 0, t, a1, a0, Y1, Y0);
      if (t + 1 >= nt) break;
      phase(0, 1, t + 1, a0, a1, Y0, Y1);
      phase(1, 1, t + 1, a1, a0, Y1, Y0);
    }
    Y1 = MFMA(BF(mm1), S1_, Y1);
    *(LDS unsigned short*)(lds + sa) = 0;
    wb += CAP;
    if (wb >= n) break;
    *(LDS u32x4*)(lds + lb + (tid & 63) * 16) = u32x4{512, 512, 512, 512};
    *(LDS u32x4*)(lds + lb + 1024 + (tid & 63) * 16) = u32x4{512, 512, 512, 512};
    asm volatile("" ::: "memory");
    window_scan();
    window_gather(true);
    window_lists();
  }
#pragma unroll
  for (int i = 0; i < 16; ++i) { Y0[i] *= -0.6931471805599453f; Y1[i] *= -0.6931471805599453f; }
  f32x16 o0, o1;
#pragma unroll
  for (int i = 0; i < 16; ++i) { o0[i] = 0.f; o1[i] = 0.f; }
  auto ufrag = [&](int f) { return *(LDS bf16x8*)(lds + L_WU + f * 1024 + l * 16); };
#pragma unroll
  for (int s = 0; s < 4; ++s) { o0 = MFMA(ufrag(2 * s), XO[s], o0); o1 = MFMA(ufrag(2 * s + 1), XO[s], o1); }
#pragma unroll
  for (int s = 0; s < 2; ++s) {
    const bf16x8 z0 = __builtin_bit_cast(bf16x8, u32x4{pk(Y0[8 * s], Y0[8 * s + 1]), pk(Y0[8 * s + 2], Y0[8 * s + 3]), pk(Y0[8 * s + 4], Y0[8 * s + 5]), pk(Y0[8 * s + 6], Y0[8 * s + 7])});
    const bf16x8 z1 = __builtin_bit_cast(bf16x8, u32x4{pk(Y1[8 * s], Y1[8 * s + 1]), pk(Y1[8 * s + 2], Y1[8 * s + 3]), pk(Y1[8 * s + 4], Y1[8 * s + 5]), pk(Y1[8 * s + 6], Y1[8 * s + 7])});
    o0 = MFMA(ufrag(8 + 2 * s), z0, o0); o1 = MFMA(ufrag(9 + 2 * s), z0, o1);
    o0 = MFMA(ufrag(12 + 2 * s), z1, o0); o1 = MFMA(ufrag(13 + 2 * s), z1, o1);
  }
#pragma unroll
  for (int q = 0; q < 4; ++q) {
    const u32x2 v0 = {pk(silu1(o0[4 * q]), silu1(o0[4 * q + 1])), pk(silu1(o0[4 * q + 2]), silu1(o0[4 * q + 3]))};
    const u32x2 v1 = {pk(silu1(o1[4 * q]), silu1(o1[4 * q + 1])), pk(silu1(o1[4 * q + 2]), silu1(o1[4 * q + 3]))};
    *(LDS u32x2*)(lds + stg + r * 128 + ((q ^ (r & 7)) << 4) + h * 8) = v0;
    *(LDS u32x2*)(lds + stg + r * 128 + (((4 + q) ^ (r & 7)) << 4) + h * 8) = v1;
  }
  asm volatile("" ::: "memory");
#pragma unroll
  for (int it = 0; it < 4; ++it) {
    const int m = l + 64 * it, row = m >> 3, p = m & 7;
    if (m < ncnt * 8) {
      const u32x4 v = *(LDS u32x4*)(lds + stg + row * 128 + ((p ^ (row & 7)) << 4));
      asm volatile("global_store_dwordx4 %0, %1, %2 sc0 sc1\n\ts_nop 1" ::"v"(m * 16), "v"(v), "s"(out + (size_t)nbase * 128) : "memory");
    }
  }
  asm volatile(".Lkm_code_end:");
}

static inline void launch_part(void* const* d_in, void* d_ws, hipStream_t stream) {
  k_part<<<NC + 5, 512, 0, stream>>>((const i32x4*)d_in[3], (const i32x4*)d_in[2], (const uint16_t*)d_in[4], (const uint16_t*)d_in[5], (char*)d_ws);
}
static inline void launch_main(void* const* d_in, void* d_out, void* d_ws, hipStream_t stream) {
  k_main<<<NB, 512, 0, stream>>>((const char*)d_in[0], (const char*)d_in[1], (const char*)d_ws, (char*)d_out);
}

extern "C" void kernel_launch(void* const* d_in, const int* in_sizes, int n_in, void* d_out, int out_size, void* d_ws, size_t ws_size,
                              hipStream_t stream) {
  launch_part(d_in, d_ws, stream);
  launch_main(d_in, d_out, d_ws, stream);
}
```
